```python
import torch

def _softmax(x, axis=-1):
    return torch.softmax(x, dim=axis)
def _gelu(x):
    return torch.nn.functional.gelu(x, approximate="tanh")
def _silu(x):
    return torch.nn.functional.silu(x)
def _sigmoid(x):
    return torch.sigmoid(x)
def _layernorm(x, axis=-1, eps=1e-5):
    m = x.mean(dim=axis, keepdim=True)
    v = x.var(dim=axis, keepdim=True, unbiased=False)
    return (x - m) / torch.sqrt(v + eps)
def _scatter_add(tgt, idx, src, axis=0):
    return tgt.clone().index_add_(axis, idx, src)
def _segment_sum(src, seg, n):
    out = torch.zeros((n,) + src.shape[1:], dtype=src.dtype)
    return out.index_add_(0, seg, src)
def _pad(x, pw):
    flat = tuple(v for pair in reversed(pw) for v in pair)
    return torch.nn.functional.pad(x, flat)

kernel_name = 'gather_scatter__hybrid_graph_tf_graphgps__graph'

def setup_inputs():

    _w = (lambda a: (lambda t: t.to(torch.bfloat16) if t.is_floating_point() else t)(torch.as_tensor(a)))
    N, deg, D = 12500, 32, 128
    E = N * deg
    src = (lambda lo, hi, *s: torch.randint(lo, hi, s))(0, N, E).to(torch.int64)
    dst = (lambda lo, hi, *s: torch.randint(lo, hi, s))(0, N, E).to(torch.int64)
    deg_cnt = torch.bincount(dst, minlength=N).to(float) + 1.0
    sc = 1.0 / float(D) ** 0.5
    return {
        "x": _w((lambda *s: torch.randn(*s, dtype=torch.bfloat16) * 0.05)(N, D)),
        "src": _w(src),
        "dst": _w(dst),
        "deg_inv": _w((1.0 / deg_cnt).to(float)),
        "w_msg": _w((lambda *s: torch.randn(*s, dtype=torch.bfloat16) * 0.4)(D, D) * sc),
        "wq": _w((lambda *s: torch.randn(*s, dtype=torch.bfloat16) * 1.6)(D, D) * sc),
        "wk": _w((lambda *s: torch.randn(*s, dtype=torch.bfloat16) * 1.6)(D, D) * sc),
        "wv": _w((lambda *s: torch.randn(*s, dtype=torch.bfloat16) * 0.2)(D, D) * sc),
        "w_out": _w((lambda *s: torch.randn(*s, dtype=torch.bfloat16) * 0.2)(D, D) * sc),
    }

def reference(x, src, dst, deg_inv, w_msg, wq, wk, wv, w_out):
    N, D = x.shape[0], x.shape[1]
    h = _layernorm(x, axis=-1)
    m_e = torch.einsum('nd,de->ne', h, w_msg)
    h_local = _segment_sum(m_e[src], dst, N) * deg_inv[:, None]
    q = torch.einsum('nd,de->ne', h, wq)
    k = torch.einsum('nd,de->ne', h, wk)
    v = torch.einsum('nd,de->ne', h, wv)
    logits = torch.einsum('id,jd->ij', q, k) / (float(D) ** 0.5)
    attn = _softmax(logits, axis=-1)
    h_global = torch.einsum('ij,jd->id', attn, v)
    y = torch.einsum('nd,de->ne', h_local + h_global, w_out)
    return x + y
```

```cpp
#include <hip/hip_runtime.h>
#include <hip/hip_bf16.h>
#include <stdint.h>

typedef __bf16   bf16x8 __attribute__((ext_vector_type(8)));
typedef __bf16   bf16x2 __attribute__((ext_vector_type(2)));
typedef float    f32x16 __attribute__((ext_vector_type(16)));
typedef float    f32x2  __attribute__((ext_vector_type(2)));
typedef float    f32x4  __attribute__((ext_vector_type(4)));
typedef unsigned u32x4  __attribute__((ext_vector_type(4)));
typedef unsigned u32x2  __attribute__((ext_vector_type(2)));
typedef short    s16x4  __attribute__((ext_vector_type(4)));
#define DI __device__ __forceinline__
#define MFMA(a, b, c) __builtin_amdgcn_mfma_f32_32x32x16_bf16((a), (b), (c), 0, 0, 0)

constexpr int N = 12500, D = 128, E = 400000;
constexpr int NB32 = (N + 31) / 32;
constexpr int NB32P = 392;
constexpr int NQB = NB32P / 8;
constexpr int NSPLIT = 5;
constexpr int CH = 3200, NCH = E / CH;
static_assert(NCH * CH == E, "chunking");
constexpr int ROWWG = (NB32 + 3) / 4;
constexpr float QSCALE = 0.12751743082459868f;
constexpr float Q2MAX = 62.0f;

constexpr size_t WS_H    = 0;
constexpr size_t WS_QF   = WS_H + (size_t)NB32P * 32 * 256;
constexpr size_t WS_FLAG = WS_QF + (size_t)NB32P * 8192;
constexpr size_t WS_W    = WS_FLAG + 2048;
constexpr size_t WS_OFF  = WS_W + 65536;
constexpr size_t WS_ENT  = WS_OFF + (size_t)NB32P * 128 * 4;
constexpr size_t WS_PO   = WS_ENT + (size_t)E * 4;
constexpr size_t WS_ML   = WS_PO + (size_t)NSPLIT * NB32P * 8192;
constexpr size_t WS_END  = WS_ML + (size_t)NSPLIT * NB32P * 32 * 8;

DI unsigned cvtpk(float lo, float hi) { f32x2 v = {lo, hi}; bf16x2 b = __builtin_convertvector(v, bf16x2); return __builtin_bit_cast(unsigned, b); }
DI float bflo(unsigned u) { return __uint_as_float(u << 16); }
DI float bfhi(unsigned u) { return __uint_as_float(u & 0xffff0000u); }
DI unsigned swap23(unsigned i) { return (i & ~12u) | ((i & 4u) << 1) | ((i & 8u) >> 1); }
DI unsigned off_a(unsigned row, unsigned ch) { return 2048u * (row >> 3) + 512u * (ch >> 2) + 64u * (row & 7) + 16u * ((ch & 3) ^ ((row >> 2) & 3)); }
DI bf16x8 pack8(const f32x16& x, int s, float sc) {
  u32x4 p = {cvtpk(x[8 * s] * sc, x[8 * s + 1] * sc), cvtpk(x[8 * s + 2] * sc, x[8 * s + 3] * sc), cvtpk(x[8 * s + 4] * sc, x[8 * s + 5] * sc), cvtpk(x[8 * s + 6] * sc, x[8 * s + 7] * sc)};
  return __builtin_bit_cast(bf16x8, p);
}
DI bf16x8 tr_frag(unsigned base, unsigned lane, unsigned c, unsigned ks) {
  const unsigned h = lane >> 5, blk = (lane >> 4) & 1, q = (lane & 15) >> 2, p0 = lane & 3, p = ((p0 & 1) << 1) | (p0 >> 1);
  const unsigned a0 = base + off_a(16 * ks + 8 * h + q, 4 * c + 2 * blk + (p >> 1)) + 8 * (p & 1);
  const unsigned a1 = base + off_a(16 * ks + 8 * h + 4 + q, 4 * c + 2 * blk + (p >> 1)) + 8 * (p & 1);
  u32x2 lo, hi;
  asm volatile("ds_read_b64_tr_b16 %0, %2\n\tds_read_b64_tr_b16 %1, %3\n\ts_waitcnt lgkmcnt(0)" : "=&v"(lo), "=&v"(hi) : "v"(a0), "v"(a1) : "memory");
  u32x4 r = {lo[0], lo[1], hi[0], hi[1]};
  return __builtin_bit_cast(bf16x8, r);
}
DI void stage128(const uint16_t* __restrict__ w, char* lds, int tid) {
#pragma unroll
  for (int i = 0; i < 8; ++i) { const unsigned n = tid + 256 * i, row = n >> 4, ch = n & 15;
    *(u32x4*)(lds + off_a(row, ch)) = *(const u32x4*)(w + row * 128 + ch * 8); }
}

__global__ __launch_bounds__(256) void k_pre(const uint16_t* __restrict__ x, const int* __restrict__ src, const int* __restrict__ dst,
                                             const uint16_t* __restrict__ w_msg, const uint16_t* __restrict__ wq, const uint16_t* __restrict__ wk,
                                             const uint16_t* __restrict__ wv, const uint16_t* __restrict__ w_out, char* __restrict__ ws) {
  __shared__ __attribute__((aligned(16))) char lds[32768];
  const int tid = threadIdx.x, lane = tid & 63, w = tid >> 6, r = lane & 31, hh = lane >> 5;
  const int bid = blockIdx.x;
  const unsigned ldsb = (unsigned)(size_t)lds;
  f32x16 zero; for (int i = 0; i < 16; ++i) zero[i] = 0.f;
  if (bid < ROWWG) {
    const int blk = bid * 4 + w, row = blk * 32 + r;
    const bool valid = row < N;
    u32x4 xf[8];
#pragma unroll
    for (int s = 0; s < 8; ++s) xf[s] = valid ? *(const u32x4*)(x + (size_t)row * 128 + 16 * s + 8 * hh) : u32x4{0, 0, 0, 0};
    stage128(wq, lds, tid);
    float sum = 0.f;
#pragma unroll
    for (int s = 0; s < 8; ++s) for (int e = 0; e < 4; ++e) sum += bflo(xf[s][e]) + bfhi(xf[s][e]);
    sum += __shfl_xor(sum, 32);
    const float mean = sum * (1.f / 128.f);
    float sq = 0.f;
#pragma unroll
    for (int s = 0; s < 8; ++s) for (int e = 0; e < 4; ++e) { const float a = bflo(xf[s][e]) - mean, b = bfhi(xf[s][e]) - mean; sq += a * a + b * b; }
    sq += __shfl_xor(sq, 32);
    const float rstd = 1.0f / sqrtf(sq * (1.f / 128.f) + 1e-5f);
    bf16x8 hf[8];
#pragma unroll
    for (int s = 0; s < 8; ++s) { u32x4 p;
      for (int e = 0; e < 4; ++e) p[e] = cvtpk((bflo(xf[s][e]) - mean) * rstd, (bfhi(xf[s][e]) - mean) * rstd);
      hf[s] = __builtin_bit_cast(bf16x8, p);
      if (valid) *(u32x4*)(ws + WS_H + (size_t)row * 256 + 32 * s + 16 * hh) = p; }
    __syncthreads();
    if (blk < NB32P) {
      bf16x8 qb[8];
#pragma unroll
      for (int eb = 0; eb < 4; ++eb) { f32x16 acc = zero;
#pragma unroll
        for (int s = 0; s < 8; ++s) acc = MFMA(tr_frag(ldsb, lane, eb, s), hf[s], acc);
        qb[2 * eb] = pack8(acc, 0, 1.f); qb[2 * eb + 1] = pack8(acc, 1, 1.f); }
      float q2 = 0.f;
#pragma unroll
      for (int db = 0; db < 4; ++db) { f32x16 acc = zero;
#pragma unroll
        for (int s = 0; s < 8; ++s) { const bf16x8 a = *(const bf16x8*)(wk + (32 * db + swap23(r)) * 128 + 16 * s + 8 * hh); acc = MFMA(a, qb[s], acc); }
#pragma unroll
        for (int s = 0; s < 2; ++s) { const bf16x8 o = pack8(acc, s, QSCALE); const u32x4 u = __builtin_bit_cast(u32x4, o);
          for (int e = 0; e < 4; ++e) q2 += bflo(u[e]) * bflo(u[e]) + bfhi(u[e]) * bfhi(u[e]);
          *(u32x4*)(ws + WS_QF + ((size_t)blk * 8 + 2 * db + s) * 1024 + lane * 16) = u; } }
      q2 += __shfl_xor(q2, 32);
      const bool unsafe = !(q2 <= Q2MAX);
      const unsigned long long any = __ballot(unsafe);
      if (lane == 0) *(unsigned*)(ws + WS_FLAG + blk * 4) = any ? 1u : 0u;
    }
  } else if (bid < ROWWG + 2) {
    const uint16_t* wa = bid == ROWWG ? wv : w_msg;
    stage128(w_out, lds, tid);
    __syncthreads();
    const int db = w;
    bf16x8 a[8];
#pragma unroll
    for (int s = 0; s < 8; ++s) a[s] = *(const bf16x8*)(wa + (32 * db + swap23(r)) * 128 + 16 * s + 8 * hh);
#pragma unroll
    for (int fb = 0; fb < 4; ++fb) { f32x16 acc = zero;
#pragma unroll
      for (int s = 0; s < 8; ++s) acc = MFMA(a[s], tr_frag(ldsb, lane, fb, s), acc);
#pragma unroll
      for (int s = 0; s < 2; ++s) *(bf16x8*)(ws + WS_W + (size_t)(bid - ROWWG) * 32768 + (fb * 8 + 2 * db + s) * 1024 + lane * 16) = pack8(acc, s, 1.f); }
  } else {
    const int c = bid - ROWWG - 2;
    unsigned* hist = (unsigned*)lds;
    unsigned* wsum = hist + 512;
    hist[tid] = 0; hist[tid + 256] = 0;
    __syncthreads();
    constexpr int PER = (CH + 255) / 256;
    int dv[PER], sv[PER]; unsigned rk[PER];
#pragma unroll
    for (int i = 0; i < PER; ++i) { const int e = tid + 256 * i; dv[i] = e < CH ? dst[c * CH + e] : -1; sv[i] = e < CH ? src[c * CH + e] : 0; }
#pragma unroll
    for (int i = 0; i < PER; ++i) rk[i] = dv[i] >= 0 ? atomicAdd(&hist[dv[i] >> 5], 1u) : 0u;
    __syncthreads();
    const unsigned c0 = hist[2 * tid], c1 = hist[2 * tid + 1];
    unsigned inc = c0 + c1;
#pragma unroll
    for (int o = 1; o < 64; o <<= 1) { const unsigned t = __shfl_up(inc, o); if (lane >= o) inc += t; }
    if (lane == 63) wsum[w] = inc;
    __syncthreads();
    unsigned base = 0; for (int i = 0; i < w; ++i) base += wsum[i];
    const unsigned st0 = base + inc - c0 - c1, st1 = st0 + c0;
    hist[2 * tid] = st0; hist[2 * tid + 1] = st1;
    unsigned* off = (unsigned*)(ws + WS_OFF);
    if (2 * tid < NB32) off[(2 * tid) * 128 + c] = st0 | (c0 << 16);
    if (2 * tid + 1 < NB32) off[(2 * tid + 1) * 128 + c] = st1 | (c1 << 16);
    __syncthreads();
    unsigned* ent = (unsigned*)(ws + WS_ENT) + (size_t)c * CH;
#pragma unroll
    for (int i = 0; i < PER; ++i) if (dv[i] >= 0) ent[hist[dv[i] >> 5] + rk[i]] = (unsigned)sv[i] | ((unsigned)(dv[i] & 31) << 16);
  }
}

DI void attn_general(const char* __restrict__ ws_ro, char* __restrict__ ws, char* lds, int qblk, int split) {
  const int tid = threadIdx.x, lane = tid & 63, w = tid >> 6, r = lane & 31, hh = lane >> 5;
  const int u0 = split * 78, u1 = split == NSPLIT - 1 ? NB32 : u0 + 78;
  char* tile = lds + w * 8704;
  f32x16 zero; for (int i = 0; i < 16; ++i) zero[i] = 0.f;
  for (int qb = 0; qb < 2; ++qb) {
    const int blk = qblk * 8 + w * 2 + qb;
    bf16x8 qf[8];
#pragma unroll
    for (int s = 0; s < 8; ++s) qf[s] = *(const bf16x8*)(ws_ro + WS_QF + ((size_t)blk * 8 + s) * 1024 + lane * 16);
    f32x16 o[4] = {zero, zero, zero, zero};
    float m = -INFINITY, l = 0.f;
    for (int u = u0; u < u1; ++u) {
#pragma unroll
      for (int i = 0; i < 8; ++i) { const int n = lane + 64 * i, kr = n >> 4, ch = n & 15, key = u * 32 + kr;
        *(u32x4*)(tile + kr * 272 + ch * 16) = key < N ? *(const u32x4*)(ws_ro + WS_H + (size_t)key * 256 + ch * 16) : u32x4{0, 0, 0, 0}; }
      __builtin_amdgcn_fence(__ATOMIC_RELEASE, "wavefront"); __builtin_amdgcn_wave_barrier(); __builtin_amdgcn_fence(__ATOMIC_ACQUIRE, "wavefront");
      f32x16 sacc = zero;
#pragma unroll
      for (int s = 0; s < 8; ++s) { const bf16x8 a = *(const bf16x8*)(tile + swap23(r) * 272 + 32 * s + 16 * hh); sacc = MFMA(a, qf[s], sacc); }
      float tm = -INFINITY;
#pragma unroll
      for (int i = 0; i < 16; ++i) { const int key = u * 32 + 16 * (i >> 3) + 8 * hh + (i & 7); if (key >= N) sacc[i] = -INFINITY; tm = fmaxf(tm, sacc[i]); }
      tm = fmaxf(tm, __shfl_xor(tm, 32));
      const float mn = fmaxf(m, tm), alpha = exp2f(m - mn);
      m = mn; float ps = 0.f;
#pragma unroll
      for (int i = 0; i < 16; ++i) { sacc[i] = exp2f(sacc[i] - mn); ps += sacc[i]; }
      l = l * alpha + ps;
      const bf16x8 p0 = pack8(sacc, 0, 1.f), p1 = pack8(sacc, 1, 1.f);
#pragma unroll
      for (int db = 0; db < 4; ++db) {
#pragma unroll
        for (int i = 0; i < 16; ++i) o[db][i] *= alpha;
#pragma unroll
        for (int ks = 0; ks < 2; ++ks) { u32x4 a;
#pragma unroll
          for (int e = 0; e < 4; ++e) { const unsigned lo = *(const uint16_t*)(tile + (16 * ks + 8 * hh + 2 * e) * 272 + (32 * db + swap23(r)) * 2);
            const unsigned hi = *(const uint16_t*)(tile + (16 * ks + 8 * hh + 2 * e + 1) * 272 + (32 * db + swap23(r)) * 2); a[e] = lo | (hi << 16); }
          o[db] = MFMA(__builtin_bit_cast(bf16x8, a), ks ? p1 : p0, o[db]); } }
      __builtin_amdgcn_fence(__ATOMIC_RELEASE, "wavefront"); __builtin_amdgcn_wave_barrier(); __builtin_amdgcn_fence(__ATOMIC_ACQUIRE, "wavefront");
    }
    l += __shfl_xor(l, 32);
#pragma unroll
    for (int db = 0; db < 4; ++db)
#pragma unroll
      for (int s = 0; s < 2; ++s) *(bf16x8*)(ws + WS_PO + (((size_t)split * NB32P + blk) * 8 + 2 * db + s) * 1024 + lane * 16) = pack8(o[db], s, 1.f);
    if (hh == 0) *(f32x2*)(ws + WS_ML + ((size_t)split * NB32P * 32 + blk * 32 + r) * 8) = f32x2{m, l};
  }
}

__global__ __launch_bounds__(256, 1) void k_attn(char* __restrict__ ws) {
  __shared__ __attribute__((aligned(16))) char lds[65536];
  const int bid = blockIdx.x, qblk = bid / NSPLIT, split = bid % NSPLIT;
  attn_general(ws, ws, lds, qblk, split);
}

constexpr int CAP = 96;
__global__ __launch_bounds__(256) void k_post(const uint16_t* __restrict__ x, const uint16_t* __restrict__ deg_inv, const char* __restrict__ ws, uint16_t* __restrict__ out) {
  __shared__ __attribute__((aligned(16))) char lds[8192 + 8704 + 32 * CAP * 2 + 128 + 16384];
  char* zf = lds;
  char* ag = lds + 8192;
  uint16_t* lst = (uint16_t*)(lds + 8192 + 8704);
  unsigned* cnt = (unsigned*)(lds + 8192 + 8704 + 32 * CAP * 2);
  float* ovf = (float*)(lds + 8192 + 8704 + 32 * CAP * 2 + 128);
  const int tid = threadIdx.x, lane = tid & 63, w = tid >> 6, r = lane & 31, hh = lane >> 5;
  const int blk = blockIdx.x;
  bf16x8 wa[16];
#pragma unroll
  for (int s = 0; s < 8; ++s) { wa[s] = *(const bf16x8*)(ws + WS_W + (w * 8 + s) * 1024 + lane * 16); wa[8 + s] = *(const bf16x8*)(ws + WS_W + 32768 + (w * 8 + s) * 1024 + lane * 16); }
  u32x4 po[NSPLIT][2]; f32x2 ml[NSPLIT];
#pragma unroll
  for (int sp = 0; sp < NSPLIT; ++sp) {
    po[sp][0] = *(const u32x4*)(ws + WS_PO + (((size_t)sp * NB32P + blk) * 8 + 2 * w) * 1024 + lane * 16);
    po[sp][1] = *(const u32x4*)(ws + WS_PO + (((size_t)sp * NB32P + blk) * 8 + 2 * w + 1) * 1024 + lane * 16);
    ml[sp] = *(const f32x2*)(ws + WS_ML + ((size_t)sp * NB32P * 32 + blk * 32 + r) * 8); }
  if (tid < 32) cnt[tid] = 0;
  for (int i = tid; i < 32 * 128; i += 256) ovf[i] = 0.f;
  __syncthreads();
  if (tid < NCH) {
    const unsigned sg = *(const unsigned*)(ws + WS_OFF + ((size_t)blk * 128 + tid) * 4);
    const unsigned st = sg & 0xffff, n = sg >> 16;
    const unsigned* ent = (const unsigned*)(ws + WS_ENT) + (size_t)tid * CH + st;
    for (unsigned i = 0; i < n; ++i) { const unsigned e = ent[i], dl = e >> 16, sc = e & 0xffff;
      const unsigned slot = atomicAdd(&cnt[dl], 1u);
      if (slot < CAP) lst[dl * CAP + slot] = (uint16_t)sc;
      else { const uint16_t* hr = (const uint16_t*)(ws + WS_H) + (size_t)sc * 128;
        for (int d = 0; d < 128; ++d) atomicAdd(&ovf[dl * 128 + d], __uint_as_float((unsigned)hr[d] << 16)); } }
  }
  {
    float mm = ml[0][0];
#pragma unroll
    for (int sp = 1; sp < NSPLIT; ++sp) mm = fmaxf(mm, ml[sp][0]);
    float wt[NSPLIT], L = 0.f;
#pragma unroll
    for (int sp = 0; sp < NSPLIT; ++sp) { wt[sp] = exp2f(ml[sp][0] - mm); L += wt[sp] * ml[sp][1]; }
    const float il = 1.f / L;
#pragma unroll
    for (int s = 0; s < 2; ++s) { u32x4 z;
#pragma unroll
      for (int e = 0; e < 4; ++e) { float lo = 0.f, hi = 0.f;
#pragma unroll
        for (int sp = 0; sp < NSPLIT; ++sp) { lo += wt[sp] * bflo(po[sp][s][e]); hi += wt[sp] * bfhi(po[sp][s][e]); }
        z[e] = cvtpk(lo * il, hi * il); }
      *(u32x4*)(zf + (2 * w + s) * 1024 + lane * 16) = z; }
  }
  __syncthreads();
  {
    const int g = tid >> 4, c = tid & 15;
    for (int rr = 0; rr < 2; ++rr) { const int lr = g * 2 + rr, row = blk * 32 + lr;
      const unsigned n = min(cnt[lr], (unsigned)CAP);
      float acc[8];
#pragma unroll
      for (int e = 0; e < 8; ++e) acc[e] = ovf[lr * 128 + c * 8 + e];
      unsigned i = 0;
      for (; i + 4 <= n; i += 4) { u32x4 v[4];
#pragma unroll
        for (int k = 0; k < 4; ++k) v[k] = *(const u32x4*)(ws + WS_H + (size_t)lst[lr * CAP + i + k] * 256 + c * 16);
#pragma unroll
        for (int k = 0; k < 4; ++k) for (int e = 0; e < 4; ++e) { acc[2 * e] += bflo(v[k][e]); acc[2 * e + 1] += bfhi(v[k][e]); } }
      for (; i < n; ++i) { const u32x4 v = *(const u32x4*)(ws + WS_H + (size_t)lst[lr * CAP + i] * 256 + c * 16);
        for (int e = 0; e < 4; ++e) { acc[2 * e] += bflo(v[e]); acc[2 * e + 1] += bfhi(v[e]); } }
      const float di = row < N ? __uint_as_float((unsigned)deg_inv[row] << 16) : 0.f;
      u32x4 p; for (int e = 0; e < 4; ++e) p[e] = cvtpk(acc[2 * e] * di, acc[2 * e + 1] * di);
      *(u32x4*)(ag + lr * 272 + c * 16) = p; }
  }
  __syncthreads();
  f32x16 acc; for (int i = 0; i < 16; ++i) acc[i] = 0.f;
#pragma unroll
  for (int s = 0; s < 8; ++s) acc = MFMA(wa[s], *(const bf16x8*)(zf + s * 1024 + lane * 16), acc);
#pragma unroll
  for (int s = 0; s < 8; ++s) acc = MFMA(wa[8 + s], *(const bf16x8*)(ag + r * 272 + 32 * s + 16 * hh), acc);
  const int row = blk * 32 + r;
  if (row < N) {
#pragma unroll
    for (int s = 0; s < 2; ++s) { const size_t o = (size_t)row * 128 + 32 * w + 16 * s + 8 * hh;
      const u32x4 xv = *(const u32x4*)(x + o); u32x4 y;
#pragma unroll
      for (int e = 0; e < 4; ++e) y[e] = cvtpk(bflo(xv[e]) + acc[8 * s + 2 * e], bfhi(xv[e]) + acc[8 * s + 2 * e + 1]);
      *(u32x4*)(out + o) = y; }
  }
}

extern "C" void kernel_launch(void* const* d_in, const int* in_sizes, int n_in, void* d_out, int out_size, void* d_ws, size_t ws_size, hipStream_t stream) {
  const uint16_t* x = (const uint16_t*)d_in[0];
  const int* src = (const int*)d_in[1];
  const int* dst = (const int*)d_in[2];
  const uint16_t* deg_inv = (const uint16_t*)d_in[3];
  const uint16_t* w_msg = (const uint16_t*)d_in[4];
  const uint16_t* wq = (const uint16_t*)d_in[5];
  const uint16_t* wk = (const uint16_t*)d_in[6];
  const uint16_t* wv = (const uint16_t*)d_in[7];
  const uint16_t* w_out = (const uint16_t*)d_in[8];
  char* ws = (char*)d_ws;
  k_pre<<<ROWWG + 2 + NCH, 256, 0, stream>>>(x, src, dst, w_msg, wq, wk, wv, w_out, ws);
  k_attn<<<NQB * NSPLIT, 256, 0, stream>>>(ws);
  k_post<<<NB32, 256, 0, stream>>>(x, deg_inv, ws, (uint16_t*)d_out);
}
```

```cpp
#include <hip/hip_runtime.h>
#include <stdint.h>
#include <type_traits>

typedef __bf16   bf16x8 __attribute__((ext_vector_type(8)));
typedef __bf16   bf16x2 __attribute__((ext_vector_type(2)));
typedef float    f32x16 __attribute__((ext_vector_type(16)));
typedef float    f32x2  __attribute__((ext_vector_type(2)));
typedef float    f32x4  __attribute__((ext_vector_type(4)));
typedef unsigned u32x4  __attribute__((ext_vector_type(4)));
typedef unsigned u32x8  __attribute__((ext_vector_type(8)));
typedef unsigned u32x16 __attribute__((ext_vector_type(16)));
typedef unsigned u32x32 __attribute__((ext_vector_type(32)));
typedef unsigned u32x2  __attribute__((ext_vector_type(2)));
typedef short    s16x4  __attribute__((ext_vector_type(4)));
typedef __amdgpu_buffer_rsrc_t srd_t;
#define DI __device__ __forceinline__
#define LDSP(T, a) ((__attribute__((address_space(3))) T*)(size_t)(a))
#define MFMA32(a, b, c) __builtin_amdgcn_mfma_f32_32x32x16_bf16((a), (b), (c), 0, 0, 0)
#define MFMA16(a, b, c) __builtin_amdgcn_mfma_f32_16x16x32_bf16(__builtin_bit_cast(bf16x8, (a)), __builtin_bit_cast(bf16x8, (b)), (c), 0, 0, 0)

#define WAVE_SYNC() do { __builtin_amdgcn_fence(__ATOMIC_RELEASE, "wavefront"); __builtin_amdgcn_wave_barrier(); __builtin_amdgcn_fence(__ATOMIC_ACQUIRE, "wavefront"); } while (0)

template <int I, int E, class F> DI void sfor(F&& f) { if constexpr (I < E) { f(std::integral_constant<int, I>{}); sfor<I + 1, E>(f); } }
#define SFOR(n, i, ...) sfor<0, n>([&](auto i##_) { constexpr int i = decltype(i##_)::value; __VA_ARGS__ })

constexpr int N = 12500, E = 400000;
constexpr int NQ = 4;
constexpr int NQB = 49, NSPLIT = 5;
constexpr int NB32 = (N + 31) / 32;
constexpr int NB16P = NQB * 16;
constexpr int USPLIT = 78;
constexpr int CH = 4000, NCH = E / CH, CHP = 4160;
constexpr int NSW = 44, RB = 288;
constexpr int GR = 13;
#define PA 4
static_assert(NCH * CH == E && NSW * RB >= NB16P * 16 && NQ == 4, "geometry");
constexpr float QSCALE = 0.12751743082459868f;
constexpr float Q2MAX = 62.0f;
constexpr float RS128 = 0.08838834764831843f;
constexpr float ALPHA = 0.09695833463767527f;

constexpr unsigned WS_H    = 4096;
constexpr unsigned WS_QF   = WS_H + NB16P * 4096;
constexpr unsigned WS_FLAG = WS_QF + NB16P * 4096;
constexpr unsigned WS_W    = WS_FLAG + 4096;
constexpr unsigned WS_OFF  = WS_W + 65536;
constexpr unsigned WS_ENT  = WS_OFF + 32768;
constexpr unsigned WS_ELL  = WS_ENT + 1671168;
constexpr unsigned WS_CNT  = WS_ELL + NSW * RB * 128;
constexpr unsigned WS_OVD  = WS_CNT + 53248;
constexpr unsigned WS_OVF  = WS_OVD + 102400;
constexpr unsigned WS_AGG  = WS_OVF + 835584;
constexpr unsigned WS_PO   = WS_AGG + NSW * RB * 256;
constexpr unsigned WS_ML   = WS_PO + NSPLIT * NB16P * 4096;
constexpr unsigned WS_SYN  = WS_ML + NSPLIT * NB16P * 128;
constexpr unsigned WS_DBG  = WS_SYN + 392 * 128;
constexpr unsigned WS_END  = WS_DBG + 65536 * 8;

DI unsigned cvtpk(float lo, float hi) { f32x2 v = {lo, hi}; bf16x2 b = __builtin_convertvector(v, bf16x2); return __builtin_bit_cast(unsigned, b); }
DI float bflo(unsigned u) { return __uint_as_float(u << 16); }
DI float bfhi(unsigned u) { return __uint_as_float(u & 0xffff0000u); }
DI unsigned swap23(unsigned i) { return (i & ~12u) | ((i & 4u) << 1) | ((i & 8u) >> 1); }
DI unsigned swap34(unsigned i) { return (i & ~24u) | ((i & 8u) << 1) | ((i & 16u) >> 1); }
DI unsigned off_a(unsigned row, unsigned ch) { return 2048u * (row >> 3) + 512u * (ch >> 2) + 64u * (row & 7) + 16u * ((ch & 3) ^ ((row >> 2) & 3)); }
DI srd_t mksrd(const void* p, unsigned bytes) { return __builtin_amdgcn_make_buffer_rsrc((void*)p, (short)0, bytes, 0x00020000); }
DI u32x4 bld4(srd_t r, unsigned voff, unsigned soff = 0) { return __builtin_amdgcn_raw_buffer_load_b128(r, voff, soff, 0); }
DI u32x2 bld2(srd_t r, unsigned voff, unsigned soff = 0) { return __builtin_amdgcn_raw_buffer_load_b64(r, voff, soff, 0); }
typedef __attribute__((address_space(1))) unsigned gu32;
#define RLX_AGENT __ATOMIC_RELAXED, __HIP_MEMORY_SCOPE_AGENT
DI void st4_wt(srd_t r, unsigned voff, const u32x4& v) { __builtin_amdgcn_raw_buffer_store_b128(v, r, voff, 0, 16); }
DI void set_flag(char* ws, unsigned i) { __hip_atomic_store((gu32*)(ws + WS_SYN + i * 128u), 1u, RLX_AGENT); }
DI unsigned get_flag(char* ws, unsigned i) { return __hip_atomic_load((gu32*)(ws + WS_SYN + i * 128u), RLX_AGENT); }
DI u32x4 ld4_dev(srd_t r, unsigned voff, unsigned soff = 0) { return __builtin_amdgcn_raw_buffer_load_b128(r, voff, soff, 16); }
DI void wait_flags(char* ws, unsigned i, unsigned f) {
  for (unsigned spins = 0; __any(f != 1u) && spins < (1u << 20); ++spins) { __builtin_amdgcn_s_sleep(1); f = get_flag(ws, i); }
  __builtin_amdgcn_fence(__ATOMIC_ACQUIRE, "wavefront");
}
DI float gsum(float v) {
  u32x2 a = __builtin_amdgcn_permlane16_swap(__float_as_uint(v), __float_as_uint(v), false, false);
  v = __uint_as_float(a[0]) + __uint_as_float(a[1]);
  a = __builtin_amdgcn_permlane32_swap(__float_as_uint(v), __float_as_uint(v), false, false);
  return __uint_as_float(a[0]) + __uint_as_float(a[1]);
}
DI u32x4 pack8(const f32x4& a, const f32x4& b) { return u32x4{cvtpk(a[0], a[1]), cvtpk(a[2], a[3]), cvtpk(b[0], b[1]), cvtpk(b[2], b[3])}; }
DI bf16x8 pack8w(const f32x16& x, int s) {
  u32x4 p = {cvtpk(x[8 * s], x[8 * s + 1]), cvtpk(x[8 * s + 2], x[8 * s + 3]), cvtpk(x[8 * s + 4], x[8 * s + 5]), cvtpk(x[8 * s + 6], x[8 * s + 7])};
  return __builtin_bit_cast(bf16x8, p);
}
DI void dma16(srd_t r, unsigned voff, unsigned ldsdst) {
  unsigned keep;
  asm volatile("s_mov_b32 %0, m0\n\ts_mov_b32 m0, %3\n\ts_nop 0\n\tbuffer_load_dwordx4 %1, %2, 0 offen lds\n\ts_mov_b32 m0, %0" : "=&s"(keep) : "v"(voff), "s"(r), "s"(ldsdst) : "memory");
}
DI u32x4 tr_frag(unsigned a0, unsigned a1, int cb, int ks) {
  const unsigned o = 8192u * ks + 512u * (cb >> 1), x = 32u * (cb & 1);
  const s16x4 lo = __builtin_amdgcn_ds_read_tr16_b64_v4i16(LDSP(s16x4, (a0 ^ x) + o)), hi = __builtin_amdgcn_ds_read_tr16_b64_v4i16(LDSP(s16x4, (a1 ^ x) + o));
  const u32x2 l = __builtin_bit_cast(u32x2, lo), h = __builtin_bit_cast(u32x2, hi);
  return u32x4{l[0], l[1], h[0], h[1]};
}

#define AUXH 16
#define AUXQ 16
#define AUXE 16
constexpr int PRE_WWG = 8;
constexpr int PRE_ROWWG = NB16P / 4;
__global__ __launch_bounds__(256) void k_pre(const uint16_t* __restrict__ x, const int* __restrict__ src, const int* __restrict__ dst,
                                             char* __restrict__ ws, const uint16_t* __restrict__ wq, const uint16_t* __restrict__ wk,
                                             const uint16_t* __restrict__ wv, const uint16_t* __restrict__ w_out, const uint16_t* __restrict__ w_msg) {
  __shared__ __attribute__((aligned(64))) char lds[98304];
  const unsigned tid = threadIdx.x, lane = tid & 63, w = __builtin_amdgcn_readfirstlane(tid >> 6), m = lane & 15, g = lane >> 4;
  const int bid = blockIdx.x;
  const unsigned ldsb = (unsigned)(size_t)lds;
  const f32x4 zero = {0.f, 0.f, 0.f, 0.f};
  const unsigned ta0 = ldsb + off_a(8 * g + (m >> 2), (m >> 1) & 1) + 8u * (m & 1), ta1 = ldsb + off_a(8 * g + 4 + (m >> 2), (m >> 1) & 1) + 8u * (m & 1);
  const unsigned prow = 32u * w + ((lane >> 2) & 7), pq = lane >> 5;
  unsigned pfd, pfe;
  { u32x4 cr_; asm volatile("s_getpc_b64 s[60:61]\nLPC%=:\n\ts_and_b32 s61, s61, 0xffff\n\ts_mov_b32 s62, LSEND-LPC%=\n\ts_mov_b32 s63, 0x20000\n\t"
    "buffer_load_dword %0, %2, s[60:63], %3 offen" : "=&v"(pfd), "=&{s[60:63]}"(cr_) : "v"(lane * 128u), "s"((w < 2 ? w : 2 + (((unsigned)blockIdx.x >> 2 & ~1u) + w) % 10u) * 8192u) : "memory", "scc"); }
  if (__builtin_expect((unsigned)(bid - PRE_ROWWG) < PRE_WWG, 0)) {
    const bool isv = bid & 1; const unsigned fp = (bid - PRE_ROWWG) >> 1;
    const uint16_t* wa = isv ? wv : w_msg;
    const srd_t ro = mksrd(w_out, 32768);
#pragma unroll
    for (unsigned i = 0; i < 8; ++i)
      dma16(ro, 256u * (prow + 8u * (i >> 1)) + 64u * (2u * (i & 1) + pq) + 16u * ((lane & 3) ^ ((2u * (i >> 1) + (g & 1)) & 3)), ldsb + 8192u * w + 1024u * i);
    u32x4 a[2][4];
#pragma unroll
    for (int o = 0; o < 2; ++o)
#pragma unroll
      for (int s = 0; s < 4; ++s) a[o][s] = *(const u32x4*)(wa + (32 * w + (isv ? 16 * o + m : 8 * (m >> 2) + 4 * o + (m & 3))) * 128 + 32 * s + 8 * g);
    asm volatile("s_waitcnt vmcnt(0)\n\ts_barrier" :: "v"(pfd) : "memory");
    f32x4 acc[2][2];
    float* red = (float*)(lds + 32768);
#pragma unroll
    for (int fb = 0; fb < 2; ++fb) { acc[fb][0] = zero; acc[fb][1] = zero;
#pragma unroll
      for (int s = 0; s < 4; ++s) { const u32x4 b = tr_frag(ta0 + 512 * fp, ta1 + 512 * fp, fb, s); acc[fb][0] = MFMA16(a[0][s], b, acc[fb][0]); acc[fb][1] = MFMA16(a[1][s], b, acc[fb][1]); }
      float cs = 0.f;
#pragma unroll
      for (int i = 0; i < 4; ++i) cs += acc[fb][0][i] + acc[fb][1][i];
      cs = gsum(cs * RS128 - ((w == 3 && g == 3) ? acc[fb][1][3] : 0.f));
      if (g == 0) red[w * 128 + fb * 16 + m] = cs; }
    __syncthreads();
#pragma unroll
    for (int fb = 0; fb < 2; ++fb) { const unsigned f = fb * 16 + m;
      const float sh = ALPHA * ((red[f] + red[128 + f]) + (red[256 + f] + red[384 + f]));
      f32x4 p = acc[fb][0] - sh, q = acc[fb][1] - sh;
      if (w == 3 && g == 3) q[3] = 0.f;
      const unsigned ofb = 2 * fp + ((f >> 2) & 1), om = 4 * ((f >> 3) & 3) + (f & 3);
      *(u32x4*)(ws + WS_W + (isv ? 0u : 32768u) + (ofb * 4 + w) * 1024u + (g * 16 + om) * 16) = pack8(p, q); }
    __builtin_amdgcn_endpgm();
  }
  const bool isrow = bid < PRE_ROWWG;
  const unsigned b16 = bid * 4 + w, row = b16 * 16 + m, c = (bid - PRE_ROWWG - PRE_WWG) * 2, eo = tid < 250 ? (c * CH + tid * 4) * 4u : 0x80000000u;
  u32x4 xf[4], dv[4], sv[8];
  asm volatile("buffer_load_dwordx4 %0, %16, %17, 0 offen\n\tbuffer_load_dwordx4 %1, %16, %17, %18 offen\n\t"
               "buffer_load_dwordx4 %2, %16, %17, %19 offen\n\tbuffer_load_dwordx4 %3, %16, %17, %20 offen\n\t"
               "s_cmp_eq_u32 %18, 64\n\ts_cbranch_scc1 LROW%=\n\t"
               "buffer_load_dwordx4 %4, %16, %17, %20 offen offset:4000\n\tbuffer_load_dwordx4 %5, %16, %17, %22 offen\n\t"
               "buffer_load_dwordx4 %6, %16, %17, %22 offen offset:4000\n\tbuffer_load_dwordx4 %7, %16, %17, %23 offen\n\t"
               "buffer_load_dwordx4 %8, %16, %21, 0 offen\n\tbuffer_load_dwordx4 %9, %16, %21, %18 offen\n\t"
               "buffer_load_dwordx4 %10, %16, %21, %19 offen\n\tbuffer_load_dwordx4 %11, %16, %21, %20 offen\n\t"
               "buffer_load_dwordx4 %12, %16, %21, %20 offen offset:4000\n\tbuffer_load_dwordx4 %13, %16, %21, %22 offen\n\t"
               "buffer_load_dwordx4 %14, %16, %21, %22 offen offset:4000\n\tbuffer_load_dwordx4 %15, %16, %21, %23 offen\nLROW%=:"
               : "=&v"(xf[0]), "=&v"(xf[1]), "=&v"(xf[2]), "=&v"(xf[3]), "=&v"(dv[0]), "=&v"(dv[1]), "=&v"(dv[2]), "=&v"(dv[3]),
                 "=&v"(sv[0]), "=&v"(sv[1]), "=&v"(sv[2]), "=&v"(sv[3]), "=&v"(sv[4]), "=&v"(sv[5]), "=&v"(sv[6]), "=&v"(sv[7])
               : "v"(isrow ? row * 256u + 16u * g : eo), "s"(mksrd(isrow ? (const void*)x : (const void*)dst, isrow ? N * 256 : E * 4)),
                 "s"(isrow ? 64 : 4000), "s"(isrow ? 128 : 8000), "s"(isrow ? 192 : 12000), "s"(mksrd(src, E * 4)), "s"(20000), "s"(28000) : "memory", "scc");
  if (!isrow) {
    unsigned* cnt = (unsigned*)lds;
    unsigned* beg = cnt + 512;
    unsigned* stg = beg + 512;
    const bool act = tid < 250;
    const srd_t rw = mksrd(ws, WS_END);
    cnt[tid] = 0; cnt[256 + tid] = 0;
    WAVE_SYNC();
    unsigned bk[32], rk[32], lr[32];
    const unsigned cb = ldsb + w * 256 + (act ? 0u : 252u);
#pragma unroll
    for (int h = 0; h < 2; ++h) {
      unsigned top;
      asm volatile("s_waitcnt vmcnt(%2)\n\ts_movk_i32 %0, %3" : "=s"(top) : "v"(pfd), "n"(12 - 4 * h), "n"(N - 1) : "memory");
#pragma unroll
      for (int i = 0; i < 16; ++i) { const unsigned d = min((h ? dv : xf)[i >> 2][i & 3], top), k = __umul24(d, 58255u) >> 24;
        bk[16 * h + i] = cb + 4 * k; lr[16 * h + i] = d - __umul24(k, RB);
        rk[16 * h + i] = __hip_atomic_fetch_add(LDSP(unsigned, bk[16 * h + i] + 1024 * h), 1u, __ATOMIC_RELAXED, __HIP_MEMORY_SCOPE_WORKGROUP);
        asm volatile("" : "+v"(lr[16 * h + i])); } }
    asm volatile("s_waitcnt vmcnt(0) lgkmcnt(0)\n\ts_barrier" : "+v"(sv[0]), "+v"(sv[1]), "+v"(sv[2]), "+v"(sv[3]), "+v"(sv[4]), "+v"(sv[5]), "+v"(sv[6]), "+v"(sv[7]) :: "memory");
#pragma unroll
    for (int h = 0; h < 2; ++h) {
      const unsigned c0 = cnt[256 * h + lane], c1 = cnt[256 * h + 64 + lane], c2 = cnt[256 * h + 128 + lane], c3 = cnt[256 * h + 192 + lane];
      const unsigned tot = c0 + c1 + c2 + c3, totp = (tot + 3) & ~3u;
      unsigned inc = totp;
      inc += __builtin_amdgcn_update_dpp(0u, inc, 0x111, 0xf, 0xf, false); inc += __builtin_amdgcn_update_dpp(0u, inc, 0x112, 0xf, 0xf, false);
      inc += __builtin_amdgcn_update_dpp(0u, inc, 0x114, 0xf, 0xf, false); inc += __builtin_amdgcn_update_dpp(0u, inc, 0x118, 0xf, 0xf, false);
      inc += __builtin_amdgcn_update_dpp(0u, inc, 0x142, 0xa, 0xf, false); inc += __builtin_amdgcn_update_dpp(0u, inc, 0x143, 0xc, 0xf, false);
      const unsigned st = inc - totp;
      beg[256 * h + tid] = ldsb + 4096 + 4 * (5120 * h + st + (w > 0 ? c0 : 0) + (w > 1 ? c1 : 0) + (w > 2 ? c2 : 0));
      __builtin_amdgcn_raw_buffer_store_b32(st, rw, w == 0 ? lane * 4 : 0x80000000u, WS_OFF + (c + h) * 256, AUXE);
      stg[5120 * h + (w && tot + w - 1 < totp ? st + tot + w - 1 : 5119)] = 0xffffu | ((RB + 1) << 18); }
    WAVE_SYNC();
#pragma unroll
    for (int i = 0; i < 32; ++i) bk[i] = *LDSP(const unsigned, bk[i] + 2048 + 1024 * (i >> 4));
#pragma unroll
    for (int i = 0; i < 32; ++i) *LDSP(unsigned, bk[i] + 4 * rk[i]) = (lr[i] << 18) + ((sv[i >> 2][i & 3] & 0xffffu) | 0x40000u);
    asm volatile("s_waitcnt lgkmcnt(0)\n\ts_barrier" ::: "memory");
#pragma unroll
    for (unsigned i = 0; i < 9; ++i) { const unsigned q = i * 256 + tid, h = q >= CHP / 4;
      __builtin_amdgcn_raw_buffer_store_b128(*(const u32x4*)(stg + (h ? q + 1280 - CHP / 4 : q) * 4), rw, q < CHP / 2 ? q * 16 : 0x80000000u, WS_ENT + c * CHP * 4, AUXE); }
    __builtin_amdgcn_endpgm();
  }
  {
    const srd_t rq = mksrd(wq, 32768), rk = mksrd(wk, 32768), rho = mksrd(ws + WS_H, N * 256);
    const unsigned pk = (unsigned)(bid + 52) / 56u, pj = (unsigned)(bid + 52) % 56u;
    asm volatile("buffer_load_dword %0, %1, %2, %3 offen" : "=&v"(pfe) : "v"(lane < 32 ? (w * 32 + lane) * 128u : 0x80000000u), "s"(mksrd(pk < 3 ? src : dst, E * 4)),
                 "s"(min(pj * 32000u + (pk & 1) * 16000u, (unsigned)E * 4)) : "memory");
#pragma unroll
    for (unsigned i = 0; i < 8; ++i)
      dma16(rq, 256u * (prow + 8u * (i >> 1)) + 64u * (2u * (i & 1) + pq) + 16u * ((lane & 3) ^ ((2u * (i >> 1) + (g & 1)) & 3)), ldsb + 8192u * w + 1024u * i);
#pragma unroll
    for (unsigned i = 0; i < 8; ++i)
      dma16(rk, 256u * (prow + 8u * (i >> 1)) + 64u * (2u * (i & 1) + pq) + 16u * ((lane & 3) ^ (i >> 1)), ldsb + 32768u + 8192u * w + 1024u * i);
    asm volatile("s_waitcnt vmcnt(17)" : "+v"(xf[0]), "+v"(xf[1]), "+v"(xf[2]), "+v"(xf[3]) :: "memory");
    float v[32], sum = 0.f;
#pragma unroll
    for (int s = 0; s < 4; ++s)
#pragma unroll
      for (int e = 0; e < 4; ++e) { v[8 * s + 2 * e] = bflo(xf[s][e]); v[8 * s + 2 * e + 1] = bfhi(xf[s][e]); sum += v[8 * s + 2 * e] + v[8 * s + 2 * e + 1]; }
    const float mean = gsum(sum) * (1.f / 128.f);
    float sq = 0.f;
#pragma unroll
    for (int i = 0; i < 32; ++i) { v[i] -= mean; sq += v[i] * v[i]; }
    const float rstd = __builtin_amdgcn_rsqf(gsum(sq) * (1.f / 128.f) + 1e-5f);
    const float hl = gsum(g == 3 ? v[31] : 0.f) * rstd * ALPHA;
    u32x4 hf[4];
#pragma unroll
    for (int s = 0; s < 4; ++s) { u32x4 pr;
#pragma unroll
      for (int e = 0; e < 4; ++e) { const float a = v[8 * s + 2 * e] * rstd, b = v[8 * s + 2 * e + 1] * rstd;
        hf[s][e] = cvtpk(a, b); pr[e] = cvtpk(a + hl, (s == 3 && e == 3 && g == 3) ? 1.0f : b + hl); }
      __builtin_amdgcn_raw_buffer_store_b128(pr, rho, row * 256u + 64 * s + 16 * g, 0, AUXH); }
    asm volatile("s_waitcnt vmcnt(%2)\n\ts_barrier" :: "v"(pfd), "v"(pfe), "n"(12) : "memory");
    u32x4 qb[4];
#pragma unroll
    for (int j = 0; j < 4; ++j) { f32x4 a0 = zero, a1 = zero;
#pragma unroll
      for (int s = 0; s < 4; ++s) a0 = MFMA16(tr_frag(ta0, ta1, 2 * j, s), hf[s], a0);
#pragma unroll
      for (int s = 0; s < 4; ++s) a1 = MFMA16(tr_frag(ta0, ta1, 2 * j + 1, s), hf[s], a1);
      qb[j] = pack8(a0, a1); }
    asm volatile("s_waitcnt vmcnt(4)\n\ts_barrier" ::: "memory");
    f32x4 qa[8], qs4 = zero;
    const unsigned ka = ldsb + 32768u + 2048u * (m >> 2) + 64u * (m & 3) + 16u * ((g >> 1) ^ (m >> 2)) + 8u * (g & 1);
#pragma unroll
    for (int db = 0; db < 8; ++db) { qa[db] = zero;
#pragma unroll
      for (int j = 0; j < 4; ++j) { const unsigned o = 8192u * (db >> 1) + 256u * (db & 1) + 512u * j;
        const u32x2 lo = *LDSP(const volatile u32x2, ka + o), hi = *LDSP(const volatile u32x2, (ka ^ 32u) + o);
        qa[db] = MFMA16((u32x4{lo[0], lo[1], hi[0], hi[1]}), qb[j], qa[db]); }
      qs4 += qa[db]; }
    const float sh = -QSCALE * ALPHA * gsum(((qs4[0] + qs4[1]) + (qs4[2] + qs4[3])) * RS128 - (g == 3 ? qa[7][3] : 0.f));
    f32x4 q4 = zero;
#pragma unroll
    for (int s = 0; s < 4; ++s) {
      f32x4 a = qa[2 * s] * QSCALE + sh, b = qa[2 * s + 1] * QSCALE + sh;
      if (s == 3 && g == 3) b[3] = 0.f;
      __builtin_amdgcn_raw_buffer_store_b128(pack8(a, b), mksrd(ws, WS_END), WS_QF + (b16 * 4 + s) * 1024u + lane * 16, 0, AUXQ);
      q4 += a * a; q4 += b * b; }
    const float q2 = (q4[0] + q4[1]) + (q4[2] + q4[3]);
    const bool unsafe = !(gsum(q2) <= Q2MAX);
    const unsigned long long any = __ballot(unsafe);
    if (lane == 0) *(unsigned*)(ws + WS_FLAG + b16 * 4) = any ? 1u : 0u;
    if (tid < 2) *(unsigned*)(ws + WS_SYN + (bid * 2 + tid) * 128u) = 0u;
  }
}

struct Idx { u32x4 a, b; };
DI Idx ld_idx(const char* list, unsigned row, unsigned rot) { return Idx{*(const u32x4*)(list + row * 128 + (rot & 7) * 16), *(const u32x4*)(list + row * 128 + ((rot + 1) & 7) * 16)}; }
DI u32x4 gload(srd_t rh, const Idx& ix, int t, unsigned n16) {
  const unsigned pk = t < 8 ? ix.a[t >> 1] : ix.b[(t - 8) >> 1]; unsigned o;
  if (t & 1) asm("v_mad_u32_u16 %0, %1, %2, %3 op_sel:[1,0,0,0]" : "=v"(o) : "v"(pk), "s"(256u), "v"(n16));
  else       asm("v_mad_u32_u16 %0, %1, %2, %3" : "=v"(o) : "v"(pk), "s"(256u), "v"(n16));
  return bld4(rh, o);
}
#define GUV 8
constexpr int GU = GUV, GB = 12;
template <int T> DI void issue_from(srd_t rh, const Idx& ix, unsigned n16, unsigned cn, u32x4 (&v)[GB]) {
  if constexpr (T < GB) { if (cn > 4u * T) { v[T] = gload(rh, ix, T, n16); issue_from<T + 1>(rh, ix, n16, cn, v); } } }
DI void row_issue(srd_t rh, const Idx& ix, unsigned n16, unsigned cn, u32x4 (&v)[GB]) {
#pragma unroll
  for (int t = 0; t < GU; ++t) v[t] = gload(rh, ix, t, n16);
  issue_from<GU>(rh, ix, n16, cn, v);
  __builtin_amdgcn_sched_barrier(0);
}
template <int T> DI void sum_from(const u32x4& sel, unsigned cn, const u32x4 (&v)[GB], f32x4& acc) {
  if constexpr (T < GB) { if (cn > 4u * T) { acc = MFMA16(sel, v[T], acc); sum_from<T + 1>(sel, cn, v, acc); } } }
DI void row_sum(const u32x4& sel, unsigned cn, const u32x4 (&v)[GB], f32x4& acc) {
#pragma unroll
  for (int t = 0; t < GU; ++t) acc = MFMA16(sel, v[t], acc);
  sum_from<GU>(sel, cn, v, acc);
  __builtin_amdgcn_sched_barrier(0);
}
DI u32x4 gather_sel(unsigned m, unsigned h) {
  const unsigned one = (m >> 3) == h ? (0x3f80u << (16 * (m & 1))) : 0u, q = (m & 7) >> 1;
  return u32x4{q == 0 ? one : 0u, q == 1 ? one : 0u, q == 2 ? one : 0u, q == 3 ? one : 0u};
}
DI void row_rest(srd_t rh, const char* ws, const u32x4& sel, const Idx& ix, unsigned row, unsigned cn, unsigned n16, unsigned g, f32x4& acc) {
  { u32x4 x[4];
#pragma unroll
    for (int t = 0; t < 4; ++t) x[t] = gload(rh, ix, GB + t, n16);
#pragma unroll
    for (int t = 0; t < 4; ++t) acc = MFMA16(sel, x[t], acc); }
  if (cn > 64) {
    __builtin_amdgcn_fence(__ATOMIC_ACQUIRE, "agent");
    const u32x2 d = __builtin_nontemporal_load((const u32x2*)(ws + WS_OVD + row * 8u + (n16 >> 8)));
    for (unsigned i = g; i - g < d[1]; i += 4) {
      const unsigned s = i < d[1] ? __builtin_nontemporal_load((const uint16_t*)(ws + WS_OVF) + d[0] + i) : 0xffffu;
      acc = MFMA16(sel, bld4(rh, s * 256u + n16), acc); } }
}

DI void sort_wave(const uint16_t* __restrict__ deg_inv, char* ws, char* lds, unsigned sw, unsigned w) {
  const unsigned lane = __builtin_amdgcn_mbcnt_hi(~0u, __builtin_amdgcn_mbcnt_lo(~0u, 0u)), m = lane & 15, g = lane >> 4;
  char* ell = lds + w * (RB * 128 + 4096);
  unsigned* cnt = (unsigned*)(ell + RB * 128 + 128);
  u32x2* seg = (u32x2*)(cnt + 640);
  unsigned* arr = (unsigned*)(lds + RB * 128 + 4080);
  if (w == 0 && lane < 3) arr[lane] = 0;
  const srd_t rw = mksrd(ws, WS_END), rh = mksrd(ws + WS_H, N * 256);
  u32x2 sg[2]; unsigned di[5];
#pragma unroll
  for (int i = 0; i < 2; ++i) sg[i] = bld2(rw, 64 * i + 4 * m + g < NCH ? WS_OFF + ((64 * i + 4 * m + g) * 64 + sw) * 4 : 0x80000000u);
#pragma unroll
  for (int i = 0; i < 5; ++i) di[i] = deg_inv[min(sw * RB + i * 64 + lane, (unsigned)N - 1)];
#pragma unroll
  for (int i = 0; i < RB * 128 / 1024; ++i) *(u32x4*)(ell + i * 1024 + lane * 16) = u32x4{~0u, ~0u, ~0u, ~0u};
#pragma unroll
  for (int i = 0; i < 5; ++i) cnt[i * 64 + lane] = (lane & 7) * 32;
  __builtin_amdgcn_sched_barrier(0);
  unsigned sb[2], sl[2];
#pragma unroll
  for (int i = 0; i < 2; ++i) { sb[i] = WS_ENT + (sg[i][0] + (64 * i + 4 * m + g) * CHP) * 4u; sl[i] = sg[i][1] - sg[i][0]; }
  {
    constexpr int NV = NCH / 2;
    u32x4 ev[NV], of[NV], ra[NV]; unsigned long long mk[NV];
    SFOR(NCH / 4, i, { const unsigned b = __builtin_amdgcn_update_dpp(0u, sb[i >> 4], 0x150 + (i & 15), 0xf, 0xf, false), l = __builtin_amdgcn_update_dpp(0u, sl[i >> 4], 0x150 + (i & 15), 0xf, 0xf, false);
      ev[2 * i] = bld4(rw, 4 * m < l ? b + 16 * m : 0x80000000u); ev[2 * i + 1] = bld4(rw, 64 + 4 * m < l ? b + 256 + 16 * m : 0x80000000u); });
#pragma unroll
    for (int i = 0; i < 2; ++i) if (64 * i + 4 * m + g < NCH) seg[64 * i + 4 * m + g] = u32x2{sb[i], sl[i]};
    asm volatile("s_waitcnt lgkmcnt(0)\n\ts_barrier" ::: "memory");
    const unsigned cb = (unsigned)(size_t)cnt - 4, eb = (unsigned)(size_t)ell - 128 - cb * 32;
#define SDWA_HI " dst_sel:DWORD dst_unused:UNUSED_PAD src0_sel:WORD_1 src1_sel:DWORD\n\t"
    SFOR(NV + 2, i, {
      if constexpr (i < NV)
        asm volatile("v_cmp_ne_u32_e64 %8, 0, %9\n\t"
                     "v_add_u32_sdwa %4, %9, %13" SDWA_HI "v_add_u32_sdwa %5, %10, %13" SDWA_HI "v_add_u32_sdwa %6, %11, %13" SDWA_HI "v_add_u32_sdwa %7, %12, %13" SDWA_HI
                     "s_mov_b64 exec, %8\n\t"
                     "ds_add_rtn_u32 %0, %4, %15\n\tds_add_rtn_u32 %1, %5, %15\n\tds_add_rtn_u32 %2, %6, %15\n\tds_add_rtn_u32 %3, %7, %15\n\t"
                     "s_mov_b64 exec, -1\n\t"
                     "v_lshl_add_u32 %4, %4, 5, %14\n\tv_lshl_add_u32 %5, %5, 5, %14\n\tv_lshl_add_u32 %6, %6, 5, %14\n\tv_lshl_add_u32 %7, %7, 5, %14"
                     : "=&v"(of[i][0]), "=&v"(of[i][1]), "=&v"(of[i][2]), "=&v"(of[i][3]), "=&v"(ra[i][0]), "=&v"(ra[i][1]), "=&v"(ra[i][2]), "=&v"(ra[i][3]), "=&s"(mk[i])
                     : "v"(ev[i][0]), "v"(ev[i][1]), "v"(ev[i][2]), "v"(ev[i][3]), "s"(cb), "s"(eb), "v"(65u) : "memory");
      if constexpr (i >= 2) { constexpr int j = i - 2;
        asm volatile("s_waitcnt lgkmcnt(%14)\n\t"
                     "v_lshrrev_b32 %0, 1, %0\n\tv_lshrrev_b32 %1, 1, %1\n\tv_lshrrev_b32 %2, 1, %2\n\tv_lshrrev_b32 %3, 1, %3\n\t"
                     "v_and_or_b32 %0, %0, %13, %4\n\tv_and_or_b32 %1, %1, %13, %5\n\tv_and_or_b32 %2, %2, %13, %6\n\tv_and_or_b32 %3, %3, %13, %7\n\t"
                     "s_mov_b64 exec, %8\n\t"
                     "ds_write_b16 %0, %9\n\tds_write_b16 %1, %10\n\tds_write_b16 %2, %11\n\tds_write_b16 %3, %12\n\t"
                     "s_mov_b64 exec, -1"
                     : "+v"(of[j][0]), "+v"(of[j][1]), "+v"(of[j][2]), "+v"(of[j][3])
                     : "v"(ra[j][0]), "v"(ra[j][1]), "v"(ra[j][2]), "v"(ra[j][3]), "s"(mk[j]), "v"(ev[j][0]), "v"(ev[j][1]), "v"(ev[j][2]), "v"(ev[j][3]), "s"(126u),
                       "n"(j == 0 || j == NV - 1 ? 8 : j == 1 || j == NV - 2 ? 12 : 15) : "memory"); } });
    asm volatile("s_waitcnt lgkmcnt(0)" ::: "memory");
  }
#pragma unroll
  for (int i = 0; i < 2; ++i)
    for (unsigned long long lm = __ballot(sl[i] > 128u); lm; lm &= lm - 1) {
      const unsigned k = __builtin_ctzll(lm), b = __builtin_amdgcn_readlane(sb[i], k), l = __builtin_amdgcn_readlane(sl[i], k);
      for (unsigned p = 128 + lane; p - lane < l; p += 64)
        if (p < l) { const unsigned e = *(const unsigned*)(ws + b + p * 4u), o = atomicAdd(&cnt[(e >> 18) - 1], 65u); *(uint16_t*)(ell + ((e >> 18) - 1) * 128 + ((o >> 1) & 126u)) = (uint16_t)e; } }
  WAVE_SYNC();
  bool ovf = false; unsigned cn[5];
#pragma unroll
  for (int i = 0; i < 5; ++i) { const unsigned r = i * 64 + lane; cn[i] = r < RB ? (cnt[r] - (lane & 7) * 32) / 65u : 0u; di[i] = min(cn[i], 65u) | (di[i] << 16); ovf |= cn[i] > 64;
    if (r < RB) __hip_atomic_store((gu32*)(ws + WS_CNT + (sw * RB + r) * 4u), di[i], RLX_AGENT); }
  if (__any(ovf)) {
    unsigned run = sg[0][0] + sg[1][0];
    for (int o = 1; o < 64; o <<= 1) run += __shfl_xor(run, o);
#pragma unroll
    for (int i = 0; i < 5; ++i) { const unsigned r = i * 64 + lane, x = cn[i] > 64 ? cn[i] - 64 : 0u;
      unsigned inc = x;
      for (int o = 1; o < 64; o <<= 1) { const unsigned t = __shfl_up(inc, o); if (lane >= (unsigned)o) inc += t; }
      cnt[320 + r] = run + inc - x; cnt[r] = 0;
      if (r < RB) __builtin_amdgcn_raw_buffer_store_b64(u32x2{run + inc - x, x}, rw, WS_OVD + (sw * RB + r) * 8u, 0, 16);
      run += __shfl(inc, 63); }
    WAVE_SYNC();
    auto put = [&](unsigned e) { if (e) { const unsigned lr = (e >> 18) - 1, r = atomicAdd(&cnt[lr], 1u);
      if (lr < RB) { if (r < 64) *(uint16_t*)(ell + lr * 128 + (((lr * 32 + 65 * r) >> 1) & 126u)) = (uint16_t)e; else __builtin_amdgcn_raw_buffer_store_b16((uint16_t)e, rw, WS_OVF + (cnt[320 + lr] + r - 64) * 2u, 0, 16); } } };
    for (int c = 0; c < NCH; c += 10) { unsigned e[20];
#pragma unroll
      for (int i = 0; i < 20; ++i) { const u32x2 d = seg[c + (i >> 1)]; const unsigned p = lane + 64 * (i & 1); e[i] = __builtin_amdgcn_raw_buffer_load_b32(rw, p < d[1] ? d[0] + p * 4u : 0x80000000u, 0, 0); }
#pragma unroll
      for (int i = 0; i < 20; ++i) put(e[i]); }
#pragma unroll
    for (int i = 0; i < 2; ++i)
      for (unsigned long long lm = __ballot(sl[i] > 128u); lm; lm &= lm - 1) {
        const unsigned k = __builtin_ctzll(lm), b = __builtin_amdgcn_readlane(sb[i], k), l = __builtin_amdgcn_readlane(sl[i], k);
        for (unsigned p = 128 + lane; p - lane < l; p += 64) put(__builtin_amdgcn_raw_buffer_load_b32(rw, p < l ? b + p * 4u : 0x80000000u, 0, 0)); }
    asm volatile("s_waitcnt vmcnt(0)" ::: "memory");
    WAVE_SYNC();
  }
  unsigned nsel = 0;
#pragma unroll
  for (int i = 0; i < 5; ++i) { const unsigned r = i * 64 + lane, row = sw * RB + r;
    const bool pick = r < RB && row < (unsigned)N && (row & 255u) % GR < PA;
    const unsigned long long bal = __ballot(pick);
    if (pick) cnt[320 + nsel + __builtin_amdgcn_mbcnt_hi(bal >> 32, __builtin_amdgcn_mbcnt_lo(bal, 0u))] = di[i] | (r << 7);
    nsel += __builtin_popcountll(bal); }
  WAVE_SYNC();
#pragma unroll 4
  for (int i = 0; i < RB * 128 / 1024; ++i) st4_wt(rw, ((sw * RB + 8 * i + (lane >> 3)) & 255u) % GR < PA ? 0x80000000u : WS_ELL + sw * (RB * 128) + i * 1024 + lane * 16, *(const u32x4*)(ell + i * 1024 + (lane >> 3) * 128 + ((lane + (lane >> 3)) & 7) * 16));
  const u32x4 sel[2] = {gather_sel(m, 0), gather_sel(m, 1)};
  u32x4 v[4][GB];
  static_assert(PA <= 6, "the results must stay behind the lists");
  auto resp = [&](unsigned q) { return q == 0 ? ell + RB * 128 : q == 4 ? (char*)seg : ell + (q - 8) * 256; };
  unsigned e = cnt[320 + min(g, nsel - 1)], en = cnt[320 + min(4 + g, nsel - 1)];
#pragma unroll
  for (int j = 0; j < 4; ++j) { const unsigned se = __builtin_amdgcn_readlane(e, 16 * j), lr = (se >> 7) & 511u; row_issue(rh, ld_idx(ell, lr, lr + 2 * g), m * 16, se & 127u, v[j]); }
  asm volatile("s_waitcnt vmcnt(%0)" :: "n"(4 * GU) : "memory");
  if (lane == 0 && atomicAdd(arr, 1u) == 3) set_flag(ws, 256 + (sw >> 2));
#pragma unroll 1
  for (unsigned q = 0; q < nsel; q += 4) {
    const unsigned enn = cnt[320 + min(q + 8 + g, nsel - 1)];
    f32x4 acc[2] = {{0.f, 0.f, 0.f, 0.f}, {0.f, 0.f, 0.f, 0.f}};
#pragma unroll
    for (int j = 0; j < 4; ++j) {
      const unsigned se = __builtin_amdgcn_readlane(e, 16 * j), sn = __builtin_amdgcn_readlane(en, 16 * j), lr = (se >> 7) & 511u, ln = (sn >> 7) & 511u;
      const Idx nx = ld_idx(ell, ln, ln + 2 * g);
      __builtin_amdgcn_sched_barrier(0);
      row_sum(sel[j >> 1], se & 127u, v[j], acc[j & 1]);
      row_issue(rh, nx, m * 16, sn & 127u, v[j]);
      if ((se & 127u) > 4 * GB) row_rest(rh, ws, sel[j >> 1], ld_idx(ell, lr, lr + 2 * g), sw * RB + lr, se & 127u, m * 16, g, acc[j & 1]); }
    u32x2 t[4];
#pragma unroll
    for (int i = 0; i < 4; ++i) t[i] = __builtin_amdgcn_permlane16_swap(__float_as_uint(acc[0][i]), __float_as_uint(acc[1][i]), false, false);
    const float dv = bfhi(e);
    *(u32x4*)(resp(q) + lane * 16) =
          (u32x4{cvtpk(__uint_as_float(t[0][0]) * dv, __uint_as_float(t[1][0]) * dv), cvtpk(__uint_as_float(t[2][0]) * dv, __uint_as_float(t[3][0]) * dv),
                 cvtpk(__uint_as_float(t[0][1]) * dv, __uint_as_float(t[1][1]) * dv), cvtpk(__uint_as_float(t[2][1]) * dv, __uint_as_float(t[3][1]) * dv)});
    e = en; en = enn;
  }
  WAVE_SYNC();
#pragma unroll 4
  for (unsigned q = 0; q < nsel; q += 4)
    st4_wt(rw, WS_AGG + (sw * RB + ((cnt[320 + min(q + g, nsel - 1)] >> 7) & 511u)) * 256u + m * 16, *(const u32x4*)(resp(q) + lane * 16));
  asm volatile("s_waitcnt vmcnt(0)" ::: "memory");
  if (lane == 0 && atomicAdd(arr + 1, 1u) == 3) set_flag(ws, 320 + (sw >> 2));
  asm volatile("LSEND:");
}

DI void attn_general(const char* ws_ro, char* ws, char* lds, int qblk, int split, int lane, int w) {
  const int r = lane & 31, hh = lane >> 5;
  const int u0 = split * USPLIT, u1 = split == NSPLIT - 1 ? NB32 : u0 + USPLIT;
  char* tile = lds + w * 8704;
  const srd_t rw = mksrd(ws, WS_END);
  f32x16 zero; for (int i = 0; i < 16; ++i) zero[i] = 0.f;
  for (int qb = 0; qb < 2; ++qb) {
    const unsigned b16 = (qblk * 4 + w) * 4 + qb * 2 + (r >> 4);
    bf16x8 qf[8];
#pragma unroll
    for (int s = 0; s < 8; ++s) qf[s] = *(const bf16x8*)(ws_ro + WS_QF + (b16 * 4 + (s >> 1)) * 1024u + ((2 * (s & 1) + hh) * 16 + (r & 15)) * 16);
    f32x16 o[4] = {zero, zero, zero, zero};
    float m = -INFINITY;
    for (int u = u0; u < u1; ++u) {
#pragma unroll
      for (int i = 0; i < 8; ++i) { const int n = lane + 64 * i, kr = n >> 4, ch = n & 15, key = u * 32 + kr;
        *(u32x4*)(tile + kr * 272 + ch * 16) = key < N ? *(const u32x4*)(ws_ro + WS_H + key * 256u + ch * 16) : u32x4{0, 0, 0, 0}; }
      __builtin_amdgcn_fence(__ATOMIC_RELEASE, "wavefront"); __builtin_amdgcn_wave_barrier(); __builtin_amdgcn_fence(__ATOMIC_ACQUIRE, "wavefront");
      f32x16 sacc = zero;
#pragma unroll
      for (int s = 0; s < 8; ++s) { const bf16x8 a = *(const bf16x8*)(tile + swap23(r) * 272 + 32 * s + 16 * hh); sacc = MFMA32(a, qf[s], sacc); }
      float tm = -INFINITY;
#pragma unroll
      for (int i = 0; i < 16; ++i) { const int key = u * 32 + 16 * (i >> 3) + 8 * hh + (i & 7); if (key >= N) sacc[i] = -INFINITY; tm = fmaxf(tm, sacc[i]); }
      tm = fmaxf(tm, __shfl_xor(tm, 32));
      const float mn = fmaxf(m, tm), alpha = exp2f(m - mn);
      m = mn;
#pragma unroll
      for (int i = 0; i < 16; ++i) sacc[i] = exp2f(sacc[i] - mn);
      const bf16x8 p0 = pack8w(sacc, 0), p1 = pack8w(sacc, 1);
#pragma unroll
      for (int db = 0; db < 4; ++db) {
#pragma unroll
        for (int i = 0; i < 16; ++i) o[db][i] *= alpha;
#pragma unroll
        for (int ks = 0; ks < 2; ++ks) { u32x4 a;
#pragma unroll
          for (int e = 0; e < 4; ++e) { const unsigned lo = *(const uint16_t*)(tile + (16 * ks + 8 * hh + 2 * e) * 272 + (32 * db + swap34(r)) * 2);
            const unsigned hi = *(const uint16_t*)(tile + (16 * ks + 8 * hh + 2 * e + 1) * 272 + (32 * db + swap34(r)) * 2); a[e] = lo | (hi << 16); }
          o[db] = MFMA32(__builtin_bit_cast(bf16x8, a), ks ? p1 : p0, o[db]); } }
      __builtin_amdgcn_fence(__ATOMIC_RELEASE, "wavefront"); __builtin_amdgcn_wave_barrier(); __builtin_amdgcn_fence(__ATOMIC_ACQUIRE, "wavefront");
    }
#pragma unroll
    for (int db = 0; db < 4; ++db)
#pragma unroll
      for (int s = 0; s < 2; ++s) st4_wt(rw, WS_PO + ((split * NB16P + b16) * 4 + db) * 1024u + ((2 * s + hh) * 16 + (r & 15)) * 16, __builtin_bit_cast(u32x4, pack8w(o[db], s)));
    if (hh) __hip_atomic_store((gu32*)(ws + WS_ML + ((split * NB16P + b16) * 16 + (r & 15)) * 4u), __float_as_uint(m), RLX_AGENT);
  }
}

#define GRID_ATTN 256
__global__ __launch_bounds__(256, 1) void k_attn(const uint16_t* __restrict__ x, const uint16_t* __restrict__ deg_inv, char* ws, uint16_t* __restrict__ out) {
  __shared__ __attribute__((aligned(128))) char lds[4 * (RB * 128) + 4 * 4096];
  unsigned bid = blockIdx.x, w = __builtin_amdgcn_readfirstlane(threadIdx.x >> 6);
  if (bid >= NQB * NSPLIT) { sort_wave(deg_inv, ws, lds, (bid - NQB * NSPLIT) * 4 + w, w); return; }
  const unsigned ldsb = (unsigned)(size_t)lds;
  constexpr int NG = GR - PA, GL = 8;
  static_assert(WS_W == 4096 + 2 * 784 * 4096 + 4096 && PA == 4 && GR == 13 && GB == 12, "layout");
#define GEOMETRY \
  const unsigned xc = bid & 7, slot = bid >> 3, s5 = slot * 205u >> 10, qblk = min(xc * 6 + s5 + (s5 == 6 ? 48u : 0u), 48u), split = slot - 5 * s5 + (s5 == 6 ? xc : 0u); \
  const unsigned i0 = (split * 4 + w) * GR, r0 = qblk * 256 + i0, nr = min((unsigned)GR, 256u - i0), lofs = WS_ELL + r0 * 128u; \
  unsigned pso[NSPLIT];        \
  _Pragma("unroll") for (unsigned i = 0; i < NSPLIT; ++i) pso[i] = (split + 1 + i < NSPLIT ? split + 1 + i : split + 1 + i - NSPLIT) * NB16P;
  unsigned done; f32x16 ac16; f32x4 ac4; u32x32 xq; u32x16 vin; u32x8 vsel; u32x2 cp;
  srd_t srdH, rw, rx, ro;
  {
    GEOMETRY
    unsigned lane = threadIdx.x & 63, m = lane & 15, g = lane >> 4, vD[4];
#pragma unroll
    for (unsigned i = 0; i < 4; ++i)
      vD[i] = 256u * (16u * w + 8u * (i >> 1) + ((lane >> 2) & 7)) + 64u * (2u * (i & 1) + (lane >> 5)) + 16u * ((lane & 3) ^ (2u * (i >> 1) + (g & 1))) + 1024u - 1024u * i;
    const unsigned lb = ldsb + 131072 + w * 8192 + 4096, oob = m < nr ? 0u : 0x80000000u;
    vin = u32x16{ldsb + off_a(8 * (m >> 2) + (m & 3), g), ldsb + off_a(8 * (m >> 2) + (m & 3) + 4, g),
                 ldsb + off_a(8 * g + (m >> 2), (m >> 1) & 1) + 8u * (m & 1), ldsb + off_a(8 * g + 4 + (m >> 2), (m >> 1) & 1) + 8u * (m & 1),
                 vD[0], vD[1], vD[2], vD[3], lane * 16u, WS_SYN + (256 + (r0 + m) / (4 * RB)) * 128u, WS_CNT + (r0 + m) * 4u, m * 16, lb + lane * 16u, lb + PA * 128 + 32 * g,
                 ((r0 + m) * 256u + 16 * g) | oob, (((i0 + m) >> 4) * 4096u + (g * 16 + ((i0 + m) & 15)) * 16u + WS_QF + qblk * 65536u) | oob};
    unsigned b16 = (qblk * 4 + w) * NQ, qoff = WS_QF + b16 * 4096u, key0 = split * USPLIT * 32u * 256u, m0b = ldsb + 4096u * w, fo = WS_FLAG + qblk * 64 + m * 4;
    asm volatile("" : "+s"(ws), "+{v[216:231]}"(vin), "+v"(fo), "+s"(qoff), "+s"(key0), "+s"(m0b));
    srdH = mksrd(ws + WS_H - 1024, N * 256 + 1024); rw = mksrd(ws, WS_END);
    asm volatile(
  "s_mov_b32 m0, %8\n\t"
  "buffer_load_dword a96, %9, %5, 0 offen\n\t"
  "buffer_load_dwordx4 v220, %4, %7 offen offset:0 lds\n\t"
  "buffer_load_dwordx4 v221, %4, %7 offen offset:1024 lds\n\t"
  "buffer_load_dwordx4 v222, %4, %7 offen offset:2048 lds\n\t"
  "buffer_load_dwordx4 v223, %4, %7 offen offset:3072 lds\n\t"
  "buffer_load_dwordx4 a[0:3], v224, %5, %6 offen offset:0\n\t"
  "buffer_load_dwordx4 a[4:7], v224, %5, %6 offen offset:1024\n\t"
  "buffer_load_dwordx4 a[8:11], v224, %5, %6 offen offset:2048\n\t"
  "buffer_load_dwordx4 a[12:15], v224, %5, %6 offen offset:3072\n\t"
  "s_add_u32 s63, %6, 0x1000\n\t"
  "buffer_load_dwordx4 a[16:19], v224, %5, s63 offen offset:0\n\t"
  "buffer_load_dwordx4 a[20:23], v224, %5, s63 offen offset:1024\n\t"
  "buffer_load_dwordx4 a[24:27], v224, %5, s63 offen offset:2048\n\t"
  "buffer_load_dwordx4 a[28:31], v224, %5, s63 offen offset:3072\n\t"
    : "+{v[216:231]}"(vin), "+v"(lane), "+s"(bid), "+s"(w)
    : "s"(srdH), "s"(rw), "s"(qoff), "s"(key0), "s"(m0b), "v"(fo)
    : "a0", "a1", "a2", "a3", "a4", "a5", "a6", "a7", "a8", "a9", "a10", "a11", "a12", "a13", "a14", "a15", "a16", "a17", "a18", "a19", "a20", "a21", "a22", "a23", "a24", "a25", "a26", "a27", "a28", "a29", "a30", "a31", "a32", "a33", "a34", "a35", "a36", "a37", "a38", "a39", "a40", "a41", "a42", "a43", "a44", "a45", "a46", "a47", "a48", "a49", "a50", "a51", "a52", "a53", "a54", "a55", "a56", "a57", "a58", "a59", "a60", "a61", "a62", "a63", "a96", "s63", "scc", "memory");
    rx = mksrd(x, N * 256); ro = mksrd(out, N * 256); m = lane & 15;
    const unsigned pooff = WS_PO + (split * NB16P + (qblk * 4 + w) * NQ) * 4096u, dbg = WS_DBG + (bid * 4 + w) * 32, ownb = 1024u * w - 16u * 4u * GR * split;
    const u32x4 si0 = {ldsb + 0x2000u + ownb, ldsb + 0x2000u + 16u * GR * w, ldsb + 131072 + w * 8192, WS_AGG + r0 * 256u};
    const u32x16 sin = {WS_PO - WS_QF + pso[0] * 4096u, WS_PO - WS_QF + pso[1] * 4096u, WS_PO - WS_QF + pso[2] * 4096u, WS_PO - WS_QF + pso[3] * 4096u,
                        ownb, w ? 0x80000000u : WS_SYN + (qblk * NSPLIT + split) * 128u, ldsb + 65536u, 0u,
                        qoff, key0, m0b, pooff, WS_W + w * 16384u, ldsb + 65536u + w * 16384u, lofs, dbg};
    const u32x4 s0 = gather_sel(m, 0), s1 = gather_sel(m, 1);
    vsel = u32x8{s0[0], s0[1], s0[2], s0[3], s1[0], s1[1], s1[2], s1[3]};
    const unsigned fa = 320 + (r0 + (lane >> 4)) / (4 * RB), fp = qblk * NSPLIT + lane;
    cp = u32x2{0u, WS_SYN + (lane < NSPLIT && lane != split ? fp : fa) * 128u};
    asm volatile(
  "s_add_u32 s63, s52, 0x2000\n\t"
  "buffer_load_dwordx4 a[32:35], v224, %8, s63 offen offset:0\n\t"
  "v_mov_b32_e32 v128, 0\n\t"
  "v_mov_b32_e32 v129, 0\n\t"
  "v_mov_b32_e32 v130, 0\n\t"
  "v_mov_b32_e32 v131, 0\n\t"
  "s_mov_b32 s91, 2\n\t"
  "buffer_load_dwordx4 a[36:39], v224, %8, s63 offen offset:1024\n\t"
  "s_add_u32 s61, s53, 0x4000\n\t"
  "s_add_u32 s62, s61, 0x84000\n\t"
  "v_mfma_f32_32x32x16_bf16 v[0:15], v[128:131], v[128:131], 0\n\t"
  "v_mfma_f32_32x32x16_bf16 v[16:31], v[128:131], v[128:131], 0\n\t"
  "v_mfma_f32_32x32x16_bf16 v[32:47], v[128:131], v[128:131], 0\n\t"
  "buffer_load_dwordx4 a[40:43], v224, %8, s63 offen offset:2048\n\t"
  "v_mfma_f32_32x32x16_bf16 v[48:63], v[128:131], v[128:131], 0\n\t"
  "v_mfma_f32_32x32x16_bf16 v[64:79], v[128:131], v[128:131], 0\n\t"
  "v_mfma_f32_32x32x16_bf16 v[80:95], v[128:131], v[128:131], 0\n\t"
  "v_mfma_f32_32x32x16_bf16 v[96:111], v[128:131], v[128:131], 0\n\t"
  "v_mfma_f32_32x32x16_bf16 v[112:127], v[128:131], v[128:131], 0\n\t"
  "buffer_load_dwordx4 a[44:47], v224, %8, s63 offen offset:3072\n\t"
  "v_mov_b32_e32 v192, v216\n\t"
  "v_mov_b32_e32 v193, v217\n\t"
  "v_xor_b32_e32 v196, 32, v218\n\t"
  "v_xor_b32_e32 v197, 32, v219\n\t"
  "s_movk_i32 s90, 0x100\n\t"
  "s_add_u32 s63, s52, 0x3000\n\t"
  "s_mov_b32 s60, 0\n\t"
  "s_mov_b32 %0, 0\n\t"
  "s_movk_i32 s72, 0x340\n\t"
  "s_add_u32 s73, s48, 0\n\t"
  "v_add_u32_e32 v202, s73, v227\n\t"
  "buffer_load_dwordx4 a[48:51], v224, %8, s63 offen offset:0\n\t"
  "v_cmp_gt_u32_e64 s[64:65], s72, v202\n\t"
  "s_add_u32 s73, s48, 256\n\t"
  "v_add_u32_e32 v202, s73, v227\n\t"
  "v_cmp_gt_u32_e64 s[66:67], s72, v202\n\t"
  "s_add_u32 s73, s48, 512\n\t"
  "buffer_load_dwordx4 a[52:55], v224, %8, s63 offen offset:1024\n\t"
  "v_add_u32_e32 v202, s73, v227\n\t"
  "v_cmp_gt_u32_e64 s[68:69], s72, v202\n\t"
  "s_add_u32 s73, s48, 768\n\t"
  "v_add_u32_e32 v202, s73, v227\n\t"
  "v_cmp_gt_u32_e64 s[70:71], s72, v202\n\t"
  "buffer_load_dwordx4 a[56:59], v224, %8, s63 offen offset:2048\n\t"
  "buffer_load_dwordx4 a[60:63], v224, %8, s63 offen offset:3072\n\t"
  "s_add_u32 m0, s54, 0x4000\n\t"
  "s_waitcnt vmcnt(16)\n\t"
  "s_barrier\n\t"
  "v_accvgpr_read_b32 v202, a96\n\t"
  "ds_read_b128 a[64:67], v192 offset:0\n\t"
  "ds_read_b128 a[68:71], v192 offset:512\n\t"
  "ds_read_b128 a[72:75], v192 offset:1024\n\t"
  "ds_read_b128 a[76:79], v192 offset:1536\n\t"
  "ds_read_b128 a[80:83], v193 offset:0\n\t"
  "ds_read_b128 a[84:87], v193 offset:512\n\t"
  "ds_read_b128 a[88:91], v193 offset:1024\n\t"
  "ds_read_b128 a[92:95], v193 offset:1536\n\t"
  "v_cmp_ne_u32_e32 vcc, 0, v202\n\t"
  "s_waitcnt lgkmcnt(0)\n\t"
  "s_cbranch_vccnz LBAIL%=\n\t"
  "s_waitcnt vmcnt(12)\n\t"
  "v_mfma_f32_16x16x32_bf16 v[128:131], a[64:67], a[0:3], 0\n\t"
  "buffer_load_dwordx4 v220, %7, s61 offen offset:0 lds\n\t"
  "v_mfma_f32_16x16x32_bf16 v[128:131], a[68:71], a[4:7], v[128:131]\n\t"
  "v_mfma_f32_16x16x32_bf16 v[128:131], a[72:75], a[8:11], v[128:131]\n\t"
  "buffer_load_dwordx4 v221, %7, s61 offen offset:1024 lds\n\t"
  "v_mfma_f32_16x16x32_bf16 v[128:131], a[76:79], a[12:15], v[128:131]\n\t"
  "v_mfma_f32_16x16x32_bf16 v[132:135], a[80:83], a[0:3], 0\n\t"
  "buffer_load_dwordx4 v222, %7, s61 offen offset:2048 lds\n\t"
  "v_mfma_f32_16x16x32_bf16 v[132:135], a[84:87], a[4:7], v[132:135]\n\t"
  "v_mfma_f32_16x16x32_bf16 v[132:135], a[88:91], a[8:11], v[132:135]\n\t"
  "buffer_load_dwordx4 v223, %7, s61 offen offset:3072 lds\n\t"
  "v_mfma_f32_16x16x32_bf16 v[132:135], a[92:95], a[12:15], v[132:135]\n\t"
  "s_add_u32 s61, s61, 0x4000\n\t"
  "s_add_u32 m0, s54, 0x8000\n\t"
  "s_waitcnt vmcnt(12)\n\t"
  "v_mfma_f32_16x16x32_bf16 v[136:139], a[64:67], a[16:19], 0\n\t"
  "v_exp_f32_e32 v128, v128\n\t"
  "v_exp_f32_e32 v129, v129\n\t"
  "v_mfma_f32_16x16x32_bf16 v[136:139], a[68:71], a[20:23], v[136:139]\n\t"
  "v_cvt_pk_bf16_f32 v160, v128, v129\n\t"
  "v_exp_f32_e32 v130, v130\n\t"
  "v_mfma_f32_16x16x32_bf16 v[136:139], a[72:75], a[24:27], v[136:139]\n\t"
  "v_exp_f32_e32 v131, v131\n\t"
  "s_nop 0\n\t"
  "v_cvt_pk_bf16_f32 v161, v130, v131\n\t"
  "v_mfma_f32_16x16x32_bf16 v[136:139], a[76:79], a[28:31], v[136:139]\n\t"
  "v_exp_f32_e32 v132, v132\n\t"
  "v_exp_f32_e32 v133, v133\n\t"
  "v_mfma_f32_16x16x32_bf16 v[140:143], a[80:83], a[16:19], 0\n\t"
  "v_cvt_pk_bf16_f32 v162, v132, v133\n\t"
  "v_exp_f32_e32 v134, v134\n\t"
  "v_mfma_f32_16x16x32_bf16 v[140:143], a[84:87], a[20:23], v[140:143]\n\t"
  "v_exp_f32_e32 v135, v135\n\t"
  "s_nop 0\n\t"
  "v_cvt_pk_bf16_f32 v163, v134, v135\n\t"
  "v_mfma_f32_16x16x32_bf16 v[140:143], a[88:91], a[24:27], v[140:143]\n\t"
  "v_mfma_f32_16x16x32_bf16 v[140:143], a[92:95], a[28:31], v[140:143]\n\t"
  "s_waitcnt vmcnt(8)\n\t"
  "v_mfma_f32_16x16x32_bf16 v[144:147], a[64:67], a[32:35], 0\n\t"
  "v_exp_f32_e32 v136, v136\n\t"
  "v_exp_f32_e32 v137, v137\n\t"
  "v_mfma_f32_16x16x32_bf16 v[144:147], a[68:71], a[36:39], v[144:147]\n\t"
  "v_cvt_pk_bf16_f32 v164, v136, v137\n\t"
  "v_exp_f32_e32 v138, v138\n\t"
  "v_mfma_f32_16x16x32_bf16 v[144:147], a[72:75], a[40:43], v[144:147]\n\t"
  "v_exp_f32_e32 v139, v139\n\t"
  "s_nop 0\n\t"
  "v_cvt_pk_bf16_f32 v165, v138, v139\n\t"
  "v_mfma_f32_16x16x32_bf16 v[144:147], a[76:79], a[44:47], v[144:147]\n\t"
  "v_exp_f32_e32 v140, v140\n\t"
  "v_exp_f32_e32 v141, v141\n\t"
  "v_mfma_f32_16x16x32_bf16 v[148:151], a[80:83], a[32:35], 0\n\t"
  "v_cvt_pk_bf16_f32 v166, v140, v141\n\t"
  "v_exp_f32_e32 v142, v142\n\t"
  "v_mfma_f32_16x16x32_bf16 v[148:151], a[84:87], a[36:39], v[148:151]\n\t"
  "v_exp_f32_e32 v143, v143\n\t"
  "s_nop 0\n\t"
  "v_cvt_pk_bf16_f32 v167, v142, v143\n\t"
  "v_mfma_f32_16x16x32_bf16 v[148:151], a[88:91], a[40:43], v[148:151]\n\t"
  "v_mfma_f32_16x16x32_bf16 v[148:151], a[92:95], a[44:47], v[148:151]\n\t"
  "s_waitcnt vmcnt(4)\n\t"
  "v_mfma_f32_16x16x32_bf16 v[152:155], a[64:67], a[48:51], 0\n\t"
  "v_exp_f32_e32 v144, v144\n\t"
  "v_exp_f32_e32 v145, v145\n\t"
  "v_mfma_f32_16x16x32_bf16 v[152:155], a[68:71], a[52:55], v[152:155]\n\t"
  "v_cvt_pk_bf16_f32 v168, v144, v145\n\t"
  "v_exp_f32_e32 v146, v146\n\t"
  "v_mfma_f32_16x16x32_bf16 v[152:155], a[72:75], a[56:59], v[152:155]\n\t"
  "v_exp_f32_e32 v147, v147\n\t"
  "s_nop 0\n\t"
  "v_cvt_pk_bf16_f32 v169, v146, v147\n\t"
  "v_mfma_f32_16x16x32_bf16 v[152:155], a[76:79], a[60:63], v[152:155]\n\t"
  "v_exp_f32_e32 v148, v148\n\t"
  "v_exp_f32_e32 v149, v149\n\t"
  "v_mfma_f32_16x16x32_bf16 v[156:159], a[80:83], a[48:51], 0\n\t"
  "v_cvt_pk_bf16_f32 v170, v148, v149\n\t"
  "v_exp_f32_e32 v150, v150\n\t"
  "v_mfma_f32_16x16x32_bf16 v[156:159], a[84:87], a[52:55], v[156:159]\n\t"
  "v_exp_f32_e32 v151, v151\n\t"
  "s_nop 0\n\t"
  "v_cvt_pk_bf16_f32 v171, v150, v151\n\t"
  "v_mfma_f32_16x16x32_bf16 v[156:159], a[88:91], a[56:59], v[156:159]\n\t"
  "v_mfma_f32_16x16x32_bf16 v[156:159], a[92:95], a[60:63], v[156:159]\n\t"
  "ds_read_b128 a[64:67], v192 offset:8192\n\t"
  "ds_read_b128 a[68:71], v192 offset:8704\n\t"
  "ds_read_b128 a[72:75], v192 offset:9216\n\t"
  "v_add_u32_e32 v194, s60, v218\n\t"
  "ds_read_b128 a[76:79], v192 offset:9728\n\t"
  "ds_read_b128 a[80:83], v193 offset:8192\n\t"
  "v_add_u32_e32 v195, s60, v219\n\t"
  "s_add_u32 s60, s60, 0x4000\n\t"
  "s_and_b32 s60, s60, 0xffff\n\t"
  "ds_read_b128 a[84:87], v193 offset:8704\n\t"
  "ds_read_b128 a[88:91], v193 offset:9216\n\t"
  "v_xor_b32_e32 v198, 32, v194\n\t"
  "ds_read_b128 a[92:95], v193 offset:9728\n\t"
  "v_xor_b32_e32 v199, 32, v195\n\t"
  "v_add_u32_e32 v192, s60, v216\n\t"
  "v_add_u32_e32 v193, s60, v217\n\t"
  "buffer_load_dwordx4 v220, %7, s61 offen offset:0 lds\n\t"
  "v_exp_f32_e32 v152, v152\n\t"
  "v_exp_f32_e32 v153, v153\n\t"
  "buffer_load_dwordx4 v221, %7, s61 offen offset:1024 lds\n\t"
  "v_cvt_pk_bf16_f32 v172, v152, v153\n\t"
  "v_exp_f32_e32 v154, v154\n\t"
  "buffer_load_dwordx4 v222, %7, s61 offen offset:2048 lds\n\t"
  "v_exp_f32_e32 v155, v155\n\t"
  "s_nop 0\n\t"
  "v_cvt_pk_bf16_f32 v173, v154, v155\n\t"
  "buffer_load_dwordx4 v223, %7, s61 offen offset:3072 lds\n\t"
  "v_exp_f32_e32 v156, v156\n\t"
  "s_add_u32 s61, s61, 0x4000\n\t"
  "s_waitcnt lgkmcnt(0)\n\t"
  "s_branch LENTRY%=\n\t"
  ".p2align 6\n\t"
  "LLOOP%=:\n\t"
  "v_mfma_f32_16x16x32_bf16 v[116:119], a[116:119], v[188:191], v[116:119]\n\t"
  "v_exp_f32_e64 v155, v155\n\t"
  "v_mfma_f32_16x16x32_bf16 v[120:123], a[120:123], v[188:191], v[120:123]\n\t"
  "v_cvt_pk_bf16_f32 v173, v154, v155\n\t"
  "v_mfma_f32_16x16x32_bf16 v[124:127], a[124:127], v[188:191], v[124:127]\n\t"
  "s_waitcnt lgkmcnt(0)\n\t"
  "v_exp_f32_e32 v156, v156\n\t"
  "LENTRY%=:\n\t"
  "v_mfma_f32_16x16x32_bf16 v[128:131], a[64:67], a[0:3], 0\n\t"
  "ds_read_b64_tr_b16 a[96:97], v194 offset:0\n\t"
  "v_mfma_f32_16x16x32_bf16 v[128:131], a[68:71], a[4:7], v[128:131]\n\t"
  "v_exp_f32_e64 v157, v157\n\t"
  "v_mfma_f32_16x16x32_bf16 v[128:131], a[72:75], a[8:11], v[128:131]\n\t"
  "ds_read_b64_tr_b16 a[98:99], v195 offset:0\n\t"
  "v_cvt_pk_bf16_f32 v174, v156, v157\n\t"
  "v_mfma_f32_16x16x32_bf16 v[128:131], a[76:79], a[12:15], v[128:131]\n\t"
  "v_exp_f32_e64 v158, v158\n\t"
  "v_mfma_f32_16x16x32_bf16 v[132:135], a[80:83], a[0:3], 0\n\t"
  "ds_read_b64_tr_b16 a[100:101], v198 offset:0\n\t"
  "ds_read_b64_tr_b16 a[102:103], v199 offset:0\n\t"
  "v_mfma_f32_16x16x32_bf16 v[132:135], a[84:87], a[4:7], v[132:135]\n\t"
  "v_exp_f32_e64 v159, v159\n\t"
  "v_mfma_f32_16x16x32_bf16 v[132:135], a[88:91], a[8:11], v[132:135]\n\t"
  "ds_read_b64_tr_b16 a[104:105], v194 offset:512\n\t"
  "v_cvt_pk_bf16_f32 v175, v158, v159\n\t"
  "v_mfma_f32_16x16x32_bf16 v[132:135], a[92:95], a[12:15], v[132:135]\n\t"
  "v_exp_f32_e64 v128, v128\n\t"
  "v_mfma_f32_16x16x32_bf16 v[136:139], a[64:67], a[16:19], 0\n\t"
  "ds_read_b64_tr_b16 a[106:107], v195 offset:512\n\t"
  "v_mfma_f32_16x16x32_bf16 v[136:139], a[68:71], a[20:23], v[136:139]\n\t"
  "v_exp_f32_e64 v129, v129\n\t"
  "v_mfma_f32_16x16x32_bf16 v[136:139], a[72:75], a[24:27], v[136:139]\n\t"
  "ds_read_b64_tr_b16 a[108:109], v198 offset:512\n\t"
  "v_cvt_pk_bf16_f32 v176, v128, v129\n\t"
  "v_mfma_f32_16x16x32_bf16 v[136:139], a[76:79], a[28:31], v[136:139]\n\t"
  "v_exp_f32_e64 v130, v130\n\t"
  "v_mfma_f32_16x16x32_bf16 v[140:143], a[80:83], a[16:19], 0\n\t"
  "ds_read_b64_tr_b16 a[110:111], v199 offset:512\n\t"
  "v_mfma_f32_16x16x32_bf16 v[140:143], a[84:87], a[20:23], v[140:143]\n\t"
  "v_exp_f32_e64 v131, v131\n\t"
  "v_mfma_f32_16x16x32_bf16 v[140:143], a[88:91], a[24:27], v[140:143]\n\t"
  "ds_read_b64_tr_b16 a[112:113], v194 offset:1024\n\t"
  "v_cvt_pk_bf16_f32 v177, v130, v131\n\t"
  "v_mfma_f32_16x16x32_bf16 v[140:143], a[92:95], a[28:31], v[140:143]\n\t"
  "v_exp_f32_e64 v132, v132\n\t"
  "v_mfma_f32_16x16x32_bf16 v[144:147], a[64:67], a[32:35], 0\n\t"
  "ds_read_b64_tr_b16 a[114:115], v195 offset:1024\n\t"
  "ds_read_b64_tr_b16 a[116:117], v198 offset:1024\n\t"
  "v_mfma_f32_16x16x32_bf16 v[144:147], a[68:71], a[36:39], v[144:147]\n\t"
  "v_exp_f32_e64 v133, v133\n\t"
  "v_mfma_f32_16x16x32_bf16 v[144:147], a[72:75], a[40:43], v[144:147]\n\t"
  "ds_read_b64_tr_b16 a[118:119], v199 offset:1024\n\t"
  "v_cvt_pk_bf16_f32 v178, v132, v133\n\t"
  "v_mfma_f32_16x16x32_bf16 v[144:147], a[76:79], a[44:47], v[144:147]\n\t"
  "v_exp_f32_e64 v134, v134\n\t"
  "v_mfma_f32_16x16x32_bf16 v[148:151], a[80:83], a[32:35], 0\n\t"
  "ds_read_b64_tr_b16 a[120:121], v194 offset:1536\n\t"
  "v_mfma_f32_16x16x32_bf16 v[148:151], a[84:87], a[36:39], v[148:151]\n\t"
  "v_exp_f32_e64 v135, v135\n\t"
  "v_mfma_f32_16x16x32_bf16 v[148:151], a[88:91], a[40:43], v[148:151]\n\t"
  "ds_read_b64_tr_b16 a[122:123], v195 offset:1536\n\t"
  "v_cvt_pk_bf16_f32 v179, v134, v135\n\t"
  "v_mfma_f32_16x16x32_bf16 v[148:151], a[92:95], a[44:47], v[148:151]\n\t"
  "v_exp_f32_e64 v136, v136\n\t"
  "v_mfma_f32_16x16x32_bf16 v[152:155], a[64:67], a[48:51], 0\n\t"
  "ds_read_b64_tr_b16 a[124:125], v198 offset:1536\n\t"
  "ds_read_b64_tr_b16 a[126:127], v199 offset:1536\n\t"
  "v_mfma_f32_16x16x32_bf16 v[152:155], a[68:71], a[52:55], v[152:155]\n\t"
  "v_exp_f32_e64 v137, v137\n\t"
  "v_mfma_f32_16x16x32_bf16 v[152:155], a[72:75], a[56:59], v[152:155]\n\t"
  "v_cvt_pk_bf16_f32 v180, v136, v137\n\t"
  "v_mfma_f32_16x16x32_bf16 v[152:155], a[76:79], a[60:63], v[152:155]\n\t"
  "v_exp_f32_e64 v138, v138\n\t"
  "v_mfma_f32_16x16x32_bf16 v[156:159], a[80:83], a[48:51], 0\n\t"
  "s_waitcnt vmcnt(4)\n\t"
  "s_barrier\n\t"
  "v_mfma_f32_16x16x32_bf16 v[156:159], a[84:87], a[52:55], v[156:159]\n\t"
  "v_exp_f32_e64 v139, v139\n\t"
  "v_mfma_f32_16x16x32_bf16 v[156:159], a[88:91], a[56:59], v[156:159]\n\t"
  "v_cvt_pk_bf16_f32 v181, v138, v139\n\t"
  "v_mfma_f32_16x16x32_bf16 v[156:159], a[92:95], a[60:63], v[156:159]\n\t"
  "s_waitcnt lgkmcnt(0)\n\t"
  "v_exp_f32_e32 v140, v140\n\t"
  "v_mfma_f32_16x16x32_bf16 v[0:3], a[96:99], v[160:163], v[0:3]\n\t"
  "ds_read_b128 a[64:67], v192 offset:0\n\t"
  "v_mfma_f32_16x16x32_bf16 v[4:7], a[100:103], v[160:163], v[4:7]\n\t"
  "s_xor_b32 s63, s60, 0x8000\n\t"
  "v_exp_f32_e64 v141, v141\n\t"
  "v_mfma_f32_16x16x32_bf16 v[8:11], a[104:107], v[160:163], v[8:11]\n\t"
  "ds_read_b128 a[68:71], v192 offset:512\n\t"
  "v_cvt_pk_bf16_f32 v182, v140, v141\n\t"
  "v_mfma_f32_16x16x32_bf16 v[12:15], a[108:111], v[160:163], v[12:15]\n\t"
  "s_add_u32 m0, s63, s54\n\t"
  "v_exp_f32_e32 v142, v142\n\t"
  "v_mfma_f32_16x16x32_bf16 v[16:19], a[112:115], v[160:163], v[16:19]\n\t"
  "ds_read_b128 a[72:75], v192 offset:1024\n\t"
  "v_mfma_f32_16x16x32_bf16 v[20:23], a[116:119], v[160:163], v[20:23]\n\t"
  "v_exp_f32_e64 v143, v143\n\t"
  "v_mfma_f32_16x16x32_bf16 v[24:27], a[120:123], v[160:163], v[24:27]\n\t"
  "ds_read_b128 a[76:79], v192 offset:1536\n\t"
  "v_cvt_pk_bf16_f32 v183, v142, v143\n\t"
  "v_mfma_f32_16x16x32_bf16 v[28:31], a[124:127], v[160:163], v[28:31]\n\t"
  "v_exp_f32_e64 v144, v144\n\t"
  "v_mfma_f32_16x16x32_bf16 v[32:35], a[96:99], v[164:167], v[32:35]\n\t"
  "ds_read_b128 a[80:83], v193 offset:0\n\t"
  "v_mfma_f32_16x16x32_bf16 v[36:39], a[100:103], v[164:167], v[36:39]\n\t"
  "v_exp_f32_e64 v145, v145\n\t"
  "v_mfma_f32_16x16x32_bf16 v[40:43], a[104:107], v[164:167], v[40:43]\n\t"
  "ds_read_b128 a[84:87], v193 offset:512\n\t"
  "v_cvt_pk_bf16_f32 v184, v144, v145\n\t"
  "v_mfma_f32_16x16x32_bf16 v[44:47], a[108:111], v[164:167], v[44:47]\n\t"
  "v_exp_f32_e64 v146, v146\n\t"
  "v_mfma_f32_16x16x32_bf16 v[48:51], a[112:115], v[164:167], v[48:51]\n\t"
  "ds_read_b128 a[88:91], v193 offset:1024\n\t"
  "v_mfma_f32_16x16x32_bf16 v[52:55], a[116:119], v[164:167], v[52:55]\n\t"
  "v_exp_f32_e64 v147, v147\n\t"
  "v_mfma_f32_16x16x32_bf16 v[56:59], a[120:123], v[164:167], v[56:59]\n\t"
  "ds_read_b128 a[92:95], v193 offset:1536\n\t"
  "v_cvt_pk_bf16_f32 v185, v146, v147\n\t"
  "v_mfma_f32_16x16x32_bf16 v[60:63], a[124:127], v[164:167], v[60:63]\n\t"
  "v_exp_f32_e64 v148, v148\n\t"
  "v_mfma_f32_16x16x32_bf16 v[64:67], a[96:99], v[168:171], v[64:67]\n\t"
  "buffer_load_dwordx4 v220, %7, s61 offen offset:0 lds\n\t"
  "v_mfma_f32_16x16x32_bf16 v[68:71], a[100:103], v[168:171], v[68:71]\n\t"
  "v_exp_f32_e64 v149, v149\n\t"
  "v_mfma_f32_16x16x32_bf16 v[72:75], a[104:107], v[168:171], v[72:75]\n\t"
  "v_cvt_pk_bf16_f32 v186, v148, v149\n\t"
  "v_mfma_f32_16x16x32_bf16 v[76:79], a[108:111], v[168:171], v[76:79]\n\t"
  "v_exp_f32_e64 v150, v150\n\t"
  "v_mfma_f32_16x16x32_bf16 v[80:83], a[112:115], v[168:171], v[80:83]\n\t"
  "v_mfma_f32_16x16x32_bf16 v[84:87], a[116:119], v[168:171], v[84:87]\n\t"
  "v_exp_f32_e64 v151, v151\n\t"
  "v_mfma_f32_16x16x32_bf16 v[88:91], a[120:123], v[168:171], v[88:91]\n\t"
  "v_cvt_pk_bf16_f32 v187, v150, v151\n\t"
  "v_mfma_f32_16x16x32_bf16 v[92:95], a[124:127], v[168:171], v[92:95]\n\t"
  "v_exp_f32_e64 v152, v152\n\t"
  "v_mfma_f32_16x16x32_bf16 v[96:99], a[96:99], v[172:175], v[96:99]\n\t"
  "buffer_load_dwordx4 v221, %7, s61 offen offset:1024 lds\n\t"
  "v_mfma_f32_16x16x32_bf16 v[100:103], a[100:103], v[172:175], v[100:103]\n\t"
  "v_exp_f32_e64 v153, v153\n\t"
  "v_mfma_f32_16x16x32_bf16 v[104:107], a[104:107], v[172:175], v[104:107]\n\t"
  "v_cvt_pk_bf16_f32 v188, v152, v153\n\t"
  "v_mfma_f32_16x16x32_bf16 v[108:111], a[108:111], v[172:175], v[108:111]\n\t"
  "v_exp_f32_e64 v154, v154\n\t"
  "v_mfma_f32_16x16x32_bf16 v[112:115], a[112:115], v[172:175], v[112:115]\n\t"
  "v_mfma_f32_16x16x32_bf16 v[116:119], a[116:119], v[172:175], v[116:119]\n\t"
  "v_exp_f32_e64 v155, v155\n\t"
  "v_mfma_f32_16x16x32_bf16 v[120:123], a[120:123], v[172:175], v[120:123]\n\t"
  "v_cvt_pk_bf16_f32 v189, v154, v155\n\t"
  "v_mfma_f32_16x16x32_bf16 v[124:127], a[124:127], v[172:175], v[124:127]\n\t"
  "s_waitcnt lgkmcnt(0)\n\t"
  "v_exp_f32_e32 v156, v156\n\t"
  "v_mfma_f32_16x16x32_bf16 v[128:131], a[64:67], a[0:3], 0\n\t"
  "ds_read_b64_tr_b16 a[96:97], v194 offset:8192\n\t"
  "v_mfma_f32_16x16x32_bf16 v[128:131], a[68:71], a[4:7], v[128:131]\n\t"
  "v_exp_f32_e64 v157, v157\n\t"
  "v_mfma_f32_16x16x32_bf16 v[128:131], a[72:75], a[8:11], v[128:131]\n\t"
  "ds_read_b64_tr_b16 a[98:99], v195 offset:8192\n\t"
  "v_cvt_pk_bf16_f32 v190, v156, v157\n\t"
  "v_mfma_f32_16x16x32_bf16 v[128:131], a[76:79], a[12:15], v[128:131]\n\t"
  "v_exp_f32_e64 v158, v158\n\t"
  "v_mfma_f32_16x16x32_bf16 v[132:135], a[80:83], a[0:3], 0\n\t"
  "ds_read_b64_tr_b16 a[100:101], v198 offset:8192\n\t"
  "ds_read_b64_tr_b16 a[102:103], v199 offset:8192\n\t"
  "v_mfma_f32_16x16x32_bf16 v[132:135], a[84:87], a[4:7], v[132:135]\n\t"
  "v_exp_f32_e64 v159, v159\n\t"
  "v_mfma_f32_16x16x32_bf16 v[132:135], a[88:91], a[8:11], v[132:135]\n\t"
  "ds_read_b64_tr_b16 a[104:105], v194 offset:8704\n\t"
  "v_cvt_pk_bf16_f32 v191, v158, v159\n\t"
  "v_mfma_f32_16x16x32_bf16 v[132:135], a[92:95], a[12:15], v[132:135]\n\t"
  "v_exp_f32_e64 v128, v128\n\t"
  "v_mfma_f32_16x16x32_bf16 v[136:139], a[64:67], a[16:19], 0\n\t"
  "ds_read_b64_tr_b16 a[106:107], v195 offset:8704\n\t"
  "v_mfma_f32_16x16x32_bf16 v[136:139], a[68:71], a[20:23], v[136:139]\n\t"
  "v_exp_f32_e64 v129, v129\n\t"
  "v_mfma_f32_16x16x32_bf16 v[136:139], a[72:75], a[24:27], v[136:139]\n\t"
  "ds_read_b64_tr_b16 a[108:109], v198 offset:8704\n\t"
  "v_cvt_pk_bf16_f32 v160, v128, v129\n\t"
  "v_mfma_f32_16x16x32_bf16 v[136:139], a[76:79], a[28:31], v[136:139]\n\t"
  "v_exp_f32_e64 v130, v130\n\t"
  "v_mfma_f32_16x16x32_bf16 v[140:143], a[80:83], a[16:19], 0\n\t"
  "ds_read_b64_tr_b16 a[110:111], v199 offset:8704\n\t"
  "v_mfma_f32_16x16x32_bf16 v[140:143], a[84:87], a[20:23], v[140:143]\n\t"
  "v_exp_f32_e64 v131, v131\n\t"
  "v_mfma_f32_16x16x32_bf16 v[140:143], a[88:91], a[24:27], v[140:143]\n\t"
  "ds_read_b64_tr_b16 a[112:113], v194 offset:9216\n\t"
  "v_cvt_pk_bf16_f32 v161, v130, v131\n\t"
  "v_mfma_f32_16x16x32_bf16 v[140:143], a[92:95], a[28:31], v[140:143]\n\t"
  "v_exp_f32_e64 v132, v132\n\t"
  "v_mfma_f32_16x16x32_bf16 v[144:147], a[64:67], a[32:35], 0\n\t"
  "ds_read_b64_tr_b16 a[114:115], v195 offset:9216\n\t"
  "ds_read_b64_tr_b16 a[116:117], v198 offset:9216\n\t"
  "v_mfma_f32_16x16x32_bf16 v[144:147], a[68:71], a[36:39], v[144:147]\n\t"
  "v_exp_f32_e64 v133, v133\n\t"
  "v_mfma_f32_16x16x32_bf16 v[144:147], a[72:75], a[40:43], v[144:147]\n\t"
  "ds_read_b64_tr_b16 a[118:119], v199 offset:9216\n\t"
  "v_cvt_pk_bf16_f32 v162, v132, v133\n\t"
  "v_mfma_f32_16x16x32_bf16 v[144:147], a[76:79], a[44:47], v[144:147]\n\t"
  "v_exp_f32_e64 v134, v134\n\t"
  "v_mfma_f32_16x16x32_bf16 v[148:151], a[80:83], a[32:35], 0\n\t"
  "ds_read_b64_tr_b16 a[120:121], v194 offset:9728\n\t"
  "v_mfma_f32_16x16x32_bf16 v[148:151], a[84:87], a[36:39], v[148:151]\n\t"
  "v_exp_f32_e64 v135, v135\n\t"
  "v_mfma_f32_16x16x32_bf16 v[148:151], a[88:91], a[40:43], v[148:151]\n\t"
  "ds_read_b64_tr_b16 a[122:123], v195 offset:9728\n\t"
  "v_cvt_pk_bf16_f32 v163, v134, v135\n\t"
  "v_mfma_f32_16x16x32_bf16 v[148:151], a[92:95], a[44:47], v[148:151]\n\t"
  "v_exp_f32_e64 v136, v136\n\t"
  "v_mfma_f32_16x16x32_bf16 v[152:155], a[64:67], a[48:51], 0\n\t"
  "ds_read_b64_tr_b16 a[124:125], v198 offset:9728\n\t"
  "ds_read_b64_tr_b16 a[126:127], v199 offset:9728\n\t"
  "v_mfma_f32_16x16x32_bf16 v[152:155], a[68:71], a[52:55], v[152:155]\n\t"
  "v_exp_f32_e64 v137, v137\n\t"
  "v_mfma_f32_16x16x32_bf16 v[152:155], a[72:75], a[56:59], v[152:155]\n\t"
  "v_cvt_pk_bf16_f32 v164, v136, v137\n\t"
  "v_mfma_f32_16x16x32_bf16 v[152:155], a[76:79], a[60:63], v[152:155]\n\t"
  "v_exp_f32_e64 v138, v138\n\t"
  "v_mfma_f32_16x16x32_bf16 v[156:159], a[80:83], a[48:51], 0\n\t"
  "v_mfma_f32_16x16x32_bf16 v[156:159], a[84:87], a[52:55], v[156:159]\n\t"
  "v_exp_f32_e64 v139, v139\n\t"
  "v_mfma_f32_16x16x32_bf16 v[156:159], a[88:91], a[56:59], v[156:159]\n\t"
  "v_cvt_pk_bf16_f32 v165, v138, v139\n\t"
  "v_mfma_f32_16x16x32_bf16 v[156:159], a[92:95], a[60:63], v[156:159]\n\t"
  "s_waitcnt lgkmcnt(0)\n\t"
  "v_exp_f32_e32 v140, v140\n\t"
  "v_mfma_f32_16x16x32_bf16 v[0:3], a[96:99], v[176:179], v[0:3]\n\t"
  "ds_read_b128 a[64:67], v192 offset:8192\n\t"
  "v_mfma_f32_16x16x32_bf16 v[4:7], a[100:103], v[176:179], v[4:7]\n\t"
  "v_exp_f32_e64 v141, v141\n\t"
  "v_mfma_f32_16x16x32_bf16 v[8:11], a[104:107], v[176:179], v[8:11]\n\t"
  "ds_read_b128 a[68:71], v192 offset:8704\n\t"
  "v_cvt_pk_bf16_f32 v166, v140, v141\n\t"
  "v_mfma_f32_16x16x32_bf16 v[12:15], a[108:111], v[176:179], v[12:15]\n\t"
  "v_exp_f32_e64 v142, v142\n\t"
  "v_mfma_f32_16x16x32_bf16 v[16:19], a[112:115], v[176:179], v[16:19]\n\t"
  "ds_read_b128 a[72:75], v192 offset:9216\n\t"
  "v_add_u32_e64 v194, s60, v218\n\t"
  "v_mfma_f32_16x16x32_bf16 v[20:23], a[116:119], v[176:179], v[20:23]\n\t"
  "v_exp_f32_e64 v143, v143\n\t"
  "v_mfma_f32_16x16x32_bf16 v[24:27], a[120:123], v[176:179], v[24:27]\n\t"
  "ds_read_b128 a[76:79], v192 offset:9728\n\t"
  "v_cvt_pk_bf16_f32 v167, v142, v143\n\t"
  "v_mfma_f32_16x16x32_bf16 v[28:31], a[124:127], v[176:179], v[28:31]\n\t"
  "v_exp_f32_e64 v144, v144\n\t"
  "v_mfma_f32_16x16x32_bf16 v[32:35], a[96:99], v[180:183], v[32:35]\n\t"
  "ds_read_b128 a[80:83], v193 offset:8192\n\t"
  "v_add_u32_e64 v195, s60, v219\n\t"
  "v_mfma_f32_16x16x32_bf16 v[36:39], a[100:103], v[180:183], v[36:39]\n\t"
  "s_add_u32 s60, s60, 0x4000\n\t"
  "v_exp_f32_e64 v145, v145\n\t"
  "v_mfma_f32_16x16x32_bf16 v[40:43], a[104:107], v[180:183], v[40:43]\n\t"
  "s_and_b32 s60, s60, 0xffff\n\t"
  "ds_read_b128 a[84:87], v193 offset:8704\n\t"
  "v_cvt_pk_bf16_f32 v168, v144, v145\n\t"
  "v_mfma_f32_16x16x32_bf16 v[44:47], a[108:111], v[180:183], v[44:47]\n\t"
  "v_exp_f32_e64 v146, v146\n\t"
  "v_mfma_f32_16x16x32_bf16 v[48:51], a[112:115], v[180:183], v[48:51]\n\t"
  "ds_read_b128 a[88:91], v193 offset:9216\n\t"
  "v_xor_b32_e64 v198, 32, v194\n\t"
  "v_mfma_f32_16x16x32_bf16 v[52:55], a[116:119], v[180:183], v[52:55]\n\t"
  "v_exp_f32_e64 v147, v147\n\t"
  "v_mfma_f32_16x16x32_bf16 v[56:59], a[120:123], v[180:183], v[56:59]\n\t"
  "ds_read_b128 a[92:95], v193 offset:9728\n\t"
  "v_cvt_pk_bf16_f32 v169, v146, v147\n\t"
  "v_mfma_f32_16x16x32_bf16 v[60:63], a[124:127], v[180:183], v[60:63]\n\t"
  "v_exp_f32_e64 v148, v148\n\t"
  "v_mfma_f32_16x16x32_bf16 v[64:67], a[96:99], v[184:187], v[64:67]\n\t"
  "buffer_load_dwordx4 v222, %7, s61 offen offset:2048 lds\n\t"
  "v_mfma_f32_16x16x32_bf16 v[68:71], a[100:103], v[184:187], v[68:71]\n\t"
  "v_exp_f32_e64 v149, v149\n\t"
  "v_mfma_f32_16x16x32_bf16 v[72:75], a[104:107], v[184:187], v[72:75]\n\t"
  "v_cvt_pk_bf16_f32 v170, v148, v149\n\t"
  "v_xor_b32_e64 v199, 32, v195\n\t"
  "v_mfma_f32_16x16x32_bf16 v[76:79], a[108:111], v[184:187], v[76:79]\n\t"
  "v_exp_f32_e64 v150, v150\n\t"
  "v_mfma_f32_16x16x32_bf16 v[80:83], a[112:115], v[184:187], v[80:83]\n\t"
  "v_add_u32_e32 v192, s60, v216\n\t"
  "v_add_u32_e32 v193, s60, v217\n\t"
  "v_mfma_f32_16x16x32_bf16 v[84:87], a[116:119], v[184:187], v[84:87]\n\t"
  "v_exp_f32_e64 v151, v151\n\t"
  "v_mfma_f32_16x16x32_bf16 v[88:91], a[120:123], v[184:187], v[88:91]\n\t"
  "v_cvt_pk_bf16_f32 v171, v150, v151\n\t"
  "v_mfma_f32_16x16x32_bf16 v[92:95], a[124:127], v[184:187], v[92:95]\n\t"
  "v_exp_f32_e64 v152, v152\n\t"
  "v_mfma_f32_16x16x32_bf16 v[96:99], a[96:99], v[188:191], v[96:99]\n\t"
  "buffer_load_dwordx4 v223, %7, s61 offen offset:3072 lds\n\t"
  "v_mfma_f32_16x16x32_bf16 v[100:103], a[100:103], v[188:191], v[100:103]\n\t"
  "s_add_u32 s61, s61, 0x4000\n\t"
  "v_exp_f32_e64 v153, v153\n\t"
  "v_mfma_f32_16x16x32_bf16 v[104:107], a[104:107], v[188:191], v[104:107]\n\t"
  "v_cvt_pk_bf16_f32 v172, v152, v153\n\t"
  "v_mfma_f32_16x16x32_bf16 v[108:111], a[108:111], v[188:191], v[108:111]\n\t"
  "s_cmp_lg_u32 s61, s62\n\t"
  "v_exp_f32_e32 v154, v154\n\t"
  "v_mfma_f32_16x16x32_bf16 v[112:115], a[112:115], v[188:191], v[112:115]\n\t"
  "s_cbranch_scc1 LLOOP%=\n\t"
  "s_sub_u32 s91, s91, 1\n\t"
  "s_cbranch_scc1 LTAIL%=\n\t"
  "s_add_u32 s62, s62, 0x4000\n\t"
  "s_cmp_eq_u32 s91, 0\n\t"
  "s_cbranch_scc1 LXB%=\n\t"
  "s_getpc_b64 s[72:73]\n\t"
  "LPC%=:\n\t"
  "s_and_b32 s73, s73, 0xffff\n\t"
  "s_mov_b32 s74, LSLOW%=-LPC%=\n\t"
  "s_mov_b32 s75, 0x20000\n\t"
  "v_lshlrev_b32_e32 v202, 3, v224\n\t"
  "s_sub_u32 s76, s56, 0x622000\n\t"
  "s_lshr_b32 s76, s76, 1\n\t"
  "buffer_load_dword v206, v202, s[72:75], s76 offen\n\t"
  "buffer_load_dword v214, v225, %8, 0 offen sc1\n\t"
  "s_branch LLOOP%=\n\t"
  "LXB%=:\n\t"
  "s_waitcnt vmcnt(4)\n\t"
  "v_cmp_ne_u32_e32 vcc, 1, v214\n\t"
  "buffer_load_dwordx4 v[202:205], v224, %8, s58 offen sc1\n\t"
  "buffer_load_dwordx4 v[206:209], v224, %8, s58 offen offset:1024 sc1\n\t"
  "buffer_load_dword v214, v226, %8, 0 offen sc1\n\t"
  "s_cmp_eq_u64 vcc, 0\n\t"
  "s_cselect_b32 %0, 1, 3\n\t"
  "s_branch LLOOP%=\n\t"
  ".p2align 3\n\t"
  "LTAIL%=:\n\t"
  "v_mfma_f32_16x16x32_bf16 v[116:119], a[116:119], v[188:191], v[116:119]\n\t"
  "v_exp_f32_e64 v155, v155\n\t"
  "v_mfma_f32_16x16x32_bf16 v[120:123], a[120:123], v[188:191], v[120:123]\n\t"
  "v_cvt_pk_bf16_f32 v173, v154, v155\n\t"
  "v_mfma_f32_16x16x32_bf16 v[124:127], a[124:127], v[188:191], v[124:127]\n\t"
  "s_waitcnt lgkmcnt(0)\n\t"
  "v_exp_f32_e32 v156, v156\n\t"
  "v_mfma_f32_16x16x32_bf16 v[128:131], a[64:67], a[0:3], 0\n\t"
  "ds_read_b64_tr_b16 a[96:97], v218 offset:16384\n\t"
  "ds_read_b64_tr_b16 a[98:99], v219 offset:16384\n\t"
  "v_mfma_f32_16x16x32_bf16 v[128:131], a[68:71], a[4:7], v[128:131]\n\t"
  "s_waitcnt vmcnt(4)\n\t"
  "v_exp_f32_e32 v157, v157\n\t"
  "v_mfma_f32_16x16x32_bf16 v[128:131], a[72:75], a[8:11], v[128:131]\n\t"
  "ds_read_b64_tr_b16 a[100:101], v196 offset:16384\n\t"
  "v_cvt_pk_bf16_f32 v174, v156, v157\n\t"
  "v_mfma_f32_16x16x32_bf16 v[128:131], a[76:79], a[12:15], v[128:131]\n\t"
  "v_exp_f32_e64 v158, v158\n\t"
  "v_mfma_f32_16x16x32_bf16 v[132:135], a[80:83], a[0:3], 0\n\t"
  "ds_write_b128 v228, v[202:205] offset:0\n\t"
  "ds_write_b128 v228, v[206:209] offset:1024\n\t"
  "v_mfma_f32_16x16x32_bf16 v[132:135], a[84:87], a[4:7], v[132:135]\n\t"
  "v_exp_f32_e64 v159, v159\n\t"
  "v_mfma_f32_16x16x32_bf16 v[132:135], a[88:91], a[8:11], v[132:135]\n\t"
  "ds_read_b64_tr_b16 a[102:103], v197 offset:16384\n\t"
  "v_cvt_pk_bf16_f32 v175, v158, v159\n\t"
  "v_mfma_f32_16x16x32_bf16 v[132:135], a[92:95], a[12:15], v[132:135]\n\t"
  "v_exp_f32_e64 v128, v128\n\t"
  "v_mfma_f32_16x16x32_bf16 v[136:139], a[64:67], a[16:19], 0\n\t"
  "ds_read_b64_tr_b16 a[104:105], v218 offset:16896\n\t"
  "ds_read_b64_tr_b16 a[106:107], v219 offset:16896\n\t"
  "v_mfma_f32_16x16x32_bf16 v[136:139], a[68:71], a[20:23], v[136:139]\n\t"
  "v_exp_f32_e64 v129, v129\n\t"
  "v_mfma_f32_16x16x32_bf16 v[136:139], a[72:75], a[24:27], v[136:139]\n\t"
  "ds_read_b64_tr_b16 a[108:109], v196 offset:16896\n\t"
  "v_cvt_pk_bf16_f32 v176, v128, v129\n\t"
  "v_mfma_f32_16x16x32_bf16 v[136:139], a[76:79], a[28:31], v[136:139]\n\t"
  "v_exp_f32_e64 v130, v130\n\t"
  "v_mfma_f32_16x16x32_bf16 v[140:143], a[80:83], a[16:19], 0\n\t"
  "ds_read_b128 v[210:213], v229 offset:256\n\t"
  "ds_read_b128 v[202:205], v229 offset:0\n\t"
  "v_mfma_f32_16x16x32_bf16 v[140:143], a[84:87], a[20:23], v[140:143]\n\t"
  "v_exp_f32_e64 v131, v131\n\t"
  "v_mfma_f32_16x16x32_bf16 v[140:143], a[88:91], a[24:27], v[140:143]\n\t"
  "ds_read_b64_tr_b16 a[110:111], v197 offset:16896\n\t"
  "v_cvt_pk_bf16_f32 v177, v130, v131\n\t"
  "v_mfma_f32_16x16x32_bf16 v[140:143], a[92:95], a[28:31], v[140:143]\n\t"
  "v_exp_f32_e64 v132, v132\n\t"
  "v_mfma_f32_16x16x32_bf16 v[144:147], a[64:67], a[32:35], 0\n\t"
  "ds_read_b64_tr_b16 a[112:113], v218 offset:17408\n\t"
  "ds_read_b64_tr_b16 a[114:115], v219 offset:17408\n\t"
  "v_mfma_f32_16x16x32_bf16 v[144:147], a[68:71], a[36:39], v[144:147]\n\t"
  "v_exp_f32_e64 v133, v133\n\t"
  "v_mfma_f32_16x16x32_bf16 v[144:147], a[72:75], a[40:43], v[144:147]\n\t"
  "ds_read_b64_tr_b16 a[116:117], v196 offset:17408\n\t"
  "v_cvt_pk_bf16_f32 v178, v132, v133\n\t"
  "v_mfma_f32_16x16x32_bf16 v[144:147], a[76:79], a[44:47], v[144:147]\n\t"
  "v_exp_f32_e64 v134, v134\n\t"
  "v_mfma_f32_16x16x32_bf16 v[148:151], a[80:83], a[32:35], 0\n\t"
  "ds_read_b128 v[206:209], v229 offset:128\n\t"
  "v_mfma_f32_16x16x32_bf16 v[148:151], a[84:87], a[36:39], v[148:151]\n\t"
  "v_exp_f32_e64 v135, v135\n\t"
  "v_mfma_f32_16x16x32_bf16 v[148:151], a[88:91], a[40:43], v[148:151]\n\t"
  "ds_read_b64_tr_b16 a[118:119], v197 offset:17408\n\t"
  "v_cvt_pk_bf16_f32 v179, v134, v135\n\t"
  "v_mfma_f32_16x16x32_bf16 v[148:151], a[92:95], a[44:47], v[148:151]\n\t"
  "v_exp_f32_e64 v136, v136\n\t"
  "v_mfma_f32_16x16x32_bf16 v[152:155], a[64:67], a[48:51], 0\n\t"
  "ds_read_b64_tr_b16 a[120:121], v218 offset:17920\n\t"
  "ds_read_b64_tr_b16 a[122:123], v219 offset:17920\n\t"
  "v_mfma_f32_16x16x32_bf16 v[152:155], a[68:71], a[52:55], v[152:155]\n\t"
  "v_exp_f32_e64 v137, v137\n\t"
  "v_mfma_f32_16x16x32_bf16 v[152:155], a[72:75], a[56:59], v[152:155]\n\t"
  "ds_read_b64_tr_b16 a[124:125], v196 offset:17920\n\t"
  "v_cvt_pk_bf16_f32 v180, v136, v137\n\t"
  "v_mfma_f32_16x16x32_bf16 v[152:155], a[76:79], a[60:63], v[152:155]\n\t"
  "v_exp_f32_e64 v138, v138\n\t"
  "v_mfma_f32_16x16x32_bf16 v[156:159], a[80:83], a[48:51], 0\n\t"
  "s_waitcnt vmcnt(7)\n\t"
  "s_barrier\n\t"
  "v_mfma_f32_16x16x32_bf16 v[156:159], a[84:87], a[52:55], v[156:159]\n\t"
  "v_exp_f32_e64 v139, v139\n\t"
  "v_mfma_f32_16x16x32_bf16 v[156:159], a[88:91], a[56:59], v[156:159]\n\t"
  "ds_read_b64_tr_b16 a[126:127], v197 offset:17920\n\t"
  "v_cvt_pk_bf16_f32 v181, v138, v139\n\t"
  "v_mfma_f32_16x16x32_bf16 v[156:159], a[92:95], a[60:63], v[156:159]\n\t"
  "s_waitcnt lgkmcnt(2)\n\t"
  "v_exp_f32_e32 v140, v140\n\t"
  "v_mfma_f32_16x16x32_bf16 v[0:3], a[96:99], v[160:163], v[0:3]\n\t"
  "s_add_u32 m0, s54, 0x0\n\t"
  "v_mad_u32_u16 v200, v202, s90, v227\n\t"
  "v_mfma_f32_16x16x32_bf16 v[4:7], a[100:103], v[160:163], v[4:7]\n\t"
  "v_exp_f32_e64 v141, v141\n\t"
  "v_mfma_f32_16x16x32_bf16 v[8:11], a[104:107], v[160:163], v[8:11]\n\t"
  "ds_read_b128 a[64:67], v216 offset:32768\n\t"
  "v_cvt_pk_bf16_f32 v182, v140, v141\n\t"
  "v_mfma_f32_16x16x32_bf16 v[12:15], a[108:111], v[160:163], v[12:15]\n\t"
  "v_exp_f32_e64 v142, v142\n\t"
  "v_mfma_f32_16x16x32_bf16 v[16:19], a[112:115], v[160:163], v[16:19]\n\t"
  "buffer_load_dwordx4 a[128:131], v200, %7, 0 offen offset:1024\n\t"
  "v_mad_u32_u16 v201, v202, s90, v227 op_sel:[1,0,0,0]\n\t"
  "v_mfma_f32_16x16x32_bf16 v[20:23], a[116:119], v[160:163], v[20:23]\n\t"
  "s_waitcnt lgkmcnt(1)\n\t"
  "v_exp_f32_e32 v143, v143\n\t"
  "v_mfma_f32_16x16x32_bf16 v[24:27], a[120:123], v[160:163], v[24:27]\n\t"
  "ds_read_b128 a[68:71], v216 offset:33280\n\t"
  "v_cvt_pk_bf16_f32 v183, v142, v143\n\t"
  "v_mfma_f32_16x16x32_bf16 v[28:31], a[124:127], v[160:163], v[28:31]\n\t"
  "v_exp_f32_e64 v144, v144\n\t"
  "v_mfma_f32_16x16x32_bf16 v[32:35], a[96:99], v[164:167], v[32:35]\n\t"
  "buffer_load_dwordx4 a[132:135], v201, %7, 0 offen offset:1024\n\t"
  "v_mad_u32_u16 v200, v203, s90, v227\n\t"
  "v_mfma_f32_16x16x32_bf16 v[36:39], a[100:103], v[164:167], v[36:39]\n\t"
  "v_exp_f32_e64 v145, v145\n\t"
  "v_mfma_f32_16x16x32_bf16 v[40:43], a[104:107], v[164:167], v[40:43]\n\t"
  "ds_read_b128 a[72:75], v216 offset:33792\n\t"
  "v_cvt_pk_bf16_f32 v184, v144, v145\n\t"
  "v_mfma_f32_16x16x32_bf16 v[44:47], a[108:111], v[164:167], v[44:47]\n\t"
  "v_exp_f32_e64 v146, v146\n\t"
  "v_mfma_f32_16x16x32_bf16 v[48:51], a[112:115], v[164:167], v[48:51]\n\t"
  "buffer_load_dwordx4 v220, %7, s61 offen offset:0 lds\n\t"
  "v_mfma_f32_16x16x32_bf16 v[52:55], a[116:119], v[164:167], v[52:55]\n\t"
  "v_exp_f32_e64 v147, v147\n\t"
  "v_mfma_f32_16x16x32_bf16 v[56:59], a[120:123], v[164:167], v[56:59]\n\t"
  "ds_read_b128 a[76:79], v216 offset:34304\n\t"
  "v_cvt_pk_bf16_f32 v185, v146, v147\n\t"
  "v_mfma_f32_16x16x32_bf16 v[60:63], a[124:127], v[164:167], v[60:63]\n\t"
  "v_exp_f32_e64 v148, v148\n\t"
  "v_mfma_f32_16x16x32_bf16 v[64:67], a[96:99], v[168:171], v[64:67]\n\t"
  "buffer_load_dwordx4 a[136:139], v200, %7, 0 offen offset:1024\n\t"
  "v_mad_u32_u16 v201, v203, s90, v227 op_sel:[1,0,0,0]\n\t"
  "v_mfma_f32_16x16x32_bf16 v[68:71], a[100:103], v[168:171], v[68:71]\n\t"
  "v_exp_f32_e64 v149, v149\n\t"
  "v_mfma_f32_16x16x32_bf16 v[72:75], a[104:107], v[168:171], v[72:75]\n\t"
  "ds_read_b128 a[80:83], v217 offset:32768\n\t"
  "v_cvt_pk_bf16_f32 v186, v148, v149\n\t"
  "v_mfma_f32_16x16x32_bf16 v[76:79], a[108:111], v[168:171], v[76:79]\n\t"
  "v_exp_f32_e64 v150, v150\n\t"
  "v_mfma_f32_16x16x32_bf16 v[80:83], a[112:115], v[168:171], v[80:83]\n\t"
  "buffer_load_dwordx4 v221, %7, s61 offen offset:1024 lds\n\t"
  "v_mfma_f32_16x16x32_bf16 v[84:87], a[116:119], v[168:171], v[84:87]\n\t"
  "v_exp_f32_e64 v151, v151\n\t"
  "v_mfma_f32_16x16x32_bf16 v[88:91], a[120:123], v[168:171], v[88:91]\n\t"
  "ds_read_b128 a[84:87], v217 offset:33280\n\t"
  "v_cvt_pk_bf16_f32 v187, v150, v151\n\t"
  "v_mfma_f32_16x16x32_bf16 v[92:95], a[124:127], v[168:171], v[92:95]\n\t"
  "v_exp_f32_e64 v152, v152\n\t"
  "v_mfma_f32_16x16x32_bf16 v[96:99], a[96:99], v[172:175], v[96:99]\n\t"
  "ds_read_b128 a[92:95], v217 offset:34304\n\t"
  "v_mfma_f32_16x16x32_bf16 v[100:103], a[100:103], v[172:175], v[100:103]\n\t"
  "v_exp_f32_e64 v153, v153\n\t"
  "v_mfma_f32_16x16x32_bf16 v[104:107], a[104:107], v[172:175], v[104:107]\n\t"
  "ds_read_b128 a[88:91], v217 offset:33792\n\t"
  "v_cvt_pk_bf16_f32 v188, v152, v153\n\t"
  "v_mfma_f32_16x16x32_bf16 v[108:111], a[108:111], v[172:175], v[108:111]\n\t"
  "v_exp_f32_e64 v154, v154\n\t"
  "v_mfma_f32_16x16x32_bf16 v[112:115], a[112:115], v[172:175], v[112:115]\n\t"
  "buffer_load_dwordx4 a[140:143], v201, %7, 0 offen offset:1024\n\t"
  "v_mad_u32_u16 v200, v204, s90, v227\n\t"
  "v_mfma_f32_16x16x32_bf16 v[116:119], a[116:119], v[172:175], v[116:119]\n\t"
  "v_exp_f32_e64 v155, v155\n\t"
  "v_mfma_f32_16x16x32_bf16 v[120:123], a[120:123], v[172:175], v[120:123]\n\t"
  "v_cvt_pk_bf16_f32 v189, v154, v155\n\t"
  "v_mfma_f32_16x16x32_bf16 v[124:127], a[124:127], v[172:175], v[124:127]\n\t"
  "s_waitcnt lgkmcnt(2)\n\t"
  "v_exp_f32_e32 v156, v156\n\t"
  "v_mfma_f32_16x16x32_bf16 v[128:131], a[64:67], a[0:3], 0\n\t"
  "ds_read_b64_tr_b16 a[96:97], v218 offset:24576\n\t"
  "ds_read_b64_tr_b16 a[98:99], v219 offset:24576\n\t"
  "v_mfma_f32_16x16x32_bf16 v[128:131], a[68:71], a[4:7], v[128:131]\n\t"
  "v_exp_f32_e64 v157, v157\n\t"
  "v_mfma_f32_16x16x32_bf16 v[128:131], a[72:75], a[8:11], v[128:131]\n\t"
  "ds_read_b64_tr_b16 a[100:101], v196 offset:24576\n\t"
  "v_cvt_pk_bf16_f32 v190, v156, v157\n\t"
  "v_mfma_f32_16x16x32_bf16 v[128:131], a[76:79], a[12:15], v[128:131]\n\t"
  "v_exp_f32_e64 v158, v158\n\t"
  "v_mfma_f32_16x16x32_bf16 v[132:135], a[80:83], a[0:3], 0\n\t"
  "buffer_load_dwordx4 a[144:147], v200, %7, 0 offen offset:1024\n\t"
  "v_mad_u32_u16 v201, v204, s90, v227 op_sel:[1,0,0,0]\n\t"
  "v_mfma_f32_16x16x32_bf16 v[132:135], a[84:87], a[4:7], v[132:135]\n\t"
  "s_waitcnt lgkmcnt(3)\n\t"
  "v_exp_f32_e32 v159, v159\n\t"
  "v_mfma_f32_16x16x32_bf16 v[132:135], a[88:91], a[8:11], v[132:135]\n\t"
  "ds_read_b64_tr_b16 a[102:103], v197 offset:24576\n\t"
  "v_cvt_pk_bf16_f32 v191, v158, v159\n\t"
  "v_mfma_f32_16x16x32_bf16 v[132:135], a[92:95], a[12:15], v[132:135]\n\t"
  "v_exp_f32_e64 v128, v128\n\t"
  "v_mfma_f32_16x16x32_bf16 v[136:139], a[64:67], a[16:19], 0\n\t"
  "ds_read_b64_tr_b16 a[104:105], v218 offset:25088\n\t"
  "ds_read_b64_tr_b16 a[106:107], v219 offset:25088\n\t"
  "v_mfma_f32_16x16x32_bf16 v[136:139], a[68:71], a[20:23], v[136:139]\n\t"
  "v_exp_f32_e64 v129, v129\n\t"
  "v_mfma_f32_16x16x32_bf16 v[136:139], a[72:75], a[24:27], v[136:139]\n\t"
  "ds_read_b64_tr_b16 a[108:109], v196 offset:25088\n\t"
  "v_cvt_pk_bf16_f32 v160, v128, v129\n\t"
  "v_mfma_f32_16x16x32_bf16 v[136:139], a[76:79], a[28:31], v[136:139]\n\t"
  "v_exp_f32_e64 v130, v130\n\t"
  "v_mfma_f32_16x16x32_bf16 v[140:143], a[80:83], a[16:19], 0\n\t"
  "buffer_load_dwordx4 a[148:151], v201, %7, 0 offen offset:1024\n\t"
  "v_mad_u32_u16 v200, v205, s90, v227\n\t"
  "v_mfma_f32_16x16x32_bf16 v[140:143], a[84:87], a[20:23], v[140:143]\n\t"
  "v_exp_f32_e64 v131, v131\n\t"
  "v_mfma_f32_16x16x32_bf16 v[140:143], a[88:91], a[24:27], v[140:143]\n\t"
  "ds_read_b64_tr_b16 a[110:111], v197 offset:25088\n\t"
  "v_cvt_pk_bf16_f32 v161, v130, v131\n\t"
  "v_mfma_f32_16x16x32_bf16 v[140:143], a[92:95], a[28:31], v[140:143]\n\t"
  "v_exp_f32_e64 v132, v132\n\t"
  "v_mfma_f32_16x16x32_bf16 v[144:147], a[64:67], a[32:35], 0\n\t"
  "ds_read_b64_tr_b16 a[112:113], v218 offset:25600\n\t"
  "ds_read_b64_tr_b16 a[114:115], v219 offset:25600\n\t"
  "v_mfma_f32_16x16x32_bf16 v[144:147], a[68:71], a[36:39], v[144:147]\n\t"
  "v_exp_f32_e64 v133, v133\n\t"
  "v_mfma_f32_16x16x32_bf16 v[144:147], a[72:75], a[40:43], v[144:147]\n\t"
  "ds_read_b64_tr_b16 a[116:117], v196 offset:25600\n\t"
  "v_cvt_pk_bf16_f32 v162, v132, v133\n\t"
  "v_mfma_f32_16x16x32_bf16 v[144:147], a[76:79], a[44:47], v[144:147]\n\t"
  "v_exp_f32_e64 v134, v134\n\t"
  "v_mfma_f32_16x16x32_bf16 v[148:151], a[80:83], a[32:35], 0\n\t"
  "buffer_load_dwordx4 a[152:155], v200, %7, 0 offen offset:1024\n\t"
  "v_mad_u32_u16 v201, v205, s90, v227 op_sel:[1,0,0,0]\n\t"
  "v_mfma_f32_16x16x32_bf16 v[148:151], a[84:87], a[36:39], v[148:151]\n\t"
  "v_exp_f32_e64 v135, v135\n\t"
  "v_mfma_f32_16x16x32_bf16 v[148:151], a[88:91], a[40:43], v[148:151]\n\t"
  "ds_read_b64_tr_b16 a[118:119], v197 offset:25600\n\t"
  "v_cvt_pk_bf16_f32 v163, v134, v135\n\t"
  "v_mfma_f32_16x16x32_bf16 v[148:151], a[92:95], a[44:47], v[148:151]\n\t"
  "v_exp_f32_e64 v136, v136\n\t"
  "v_mfma_f32_16x16x32_bf16 v[152:155], a[64:67], a[48:51], 0\n\t"
  "ds_read_b64_tr_b16 a[120:121], v218 offset:26112\n\t"
  "ds_read_b64_tr_b16 a[122:123], v219 offset:26112\n\t"
  "v_mfma_f32_16x16x32_bf16 v[152:155], a[68:71], a[52:55], v[152:155]\n\t"
  "v_exp_f32_e64 v137, v137\n\t"
  "v_mfma_f32_16x16x32_bf16 v[152:155], a[72:75], a[56:59], v[152:155]\n\t"
  "ds_read_b64_tr_b16 a[124:125], v196 offset:26112\n\t"
  "v_cvt_pk_bf16_f32 v164, v136, v137\n\t"
  "v_mfma_f32_16x16x32_bf16 v[152:155], a[76:79], a[60:63], v[152:155]\n\t"
  "v_exp_f32_e64 v138, v138\n\t"
  "v_mfma_f32_16x16x32_bf16 v[156:159], a[80:83], a[48:51], 0\n\t"
  "buffer_load_dwordx4 a[156:159], v201, %7, 0 offen offset:1024\n\t"
  "v_mad_u32_u16 v200, v206, s90, v227\n\t"
  "v_mfma_f32_16x16x32_bf16 v[156:159], a[84:87], a[52:55], v[156:159]\n\t"
  "v_exp_f32_e64 v139, v139\n\t"
  "v_mfma_f32_16x16x32_bf16 v[156:159], a[88:91], a[56:59], v[156:159]\n\t"
  "ds_read_b64_tr_b16 a[126:127], v197 offset:26112\n\t"
  "v_cvt_pk_bf16_f32 v165, v138, v139\n\t"
  "v_mfma_f32_16x16x32_bf16 v[156:159], a[92:95], a[60:63], v[156:159]\n\t"
  "s_waitcnt lgkmcnt(2)\n\t"
  "v_exp_f32_e32 v140, v140\n\t"
  "v_mfma_f32_16x16x32_bf16 v[0:3], a[96:99], v[176:179], v[0:3]\n\t"
  "buffer_load_dwordx4 a[160:163], v200, %7, 0 offen offset:1024\n\t"
  "v_mad_u32_u16 v201, v206, s90, v227 op_sel:[1,0,0,0]\n\t"
  "v_mfma_f32_16x16x32_bf16 v[4:7], a[100:103], v[176:179], v[4:7]\n\t"
  "v_exp_f32_e64 v141, v141\n\t"
  "v_mfma_f32_16x16x32_bf16 v[8:11], a[104:107], v[176:179], v[8:11]\n\t"
  "ds_read_b128 a[64:67], v216 offset:40960\n\t"
  "v_cvt_pk_bf16_f32 v166, v140, v141\n\t"
  "v_mfma_f32_16x16x32_bf16 v[12:15], a[108:111], v[176:179], v[12:15]\n\t"
  "v_exp_f32_e64 v142, v142\n\t"
  "v_mfma_f32_16x16x32_bf16 v[16:19], a[112:115], v[176:179], v[16:19]\n\t"
  "buffer_load_dwordx4 a[164:167], v201, %7, 0 offen offset:1024\n\t"
  "v_mad_u32_u16 v200, v207, s90, v227\n\t"
  "v_mfma_f32_16x16x32_bf16 v[20:23], a[116:119], v[176:179], v[20:23]\n\t"
  "s_waitcnt lgkmcnt(1)\n\t"
  "v_exp_f32_e32 v143, v143\n\t"
  "v_mfma_f32_16x16x32_bf16 v[24:27], a[120:123], v[176:179], v[24:27]\n\t"
  "ds_read_b128 a[68:71], v216 offset:41472\n\t"
  "v_cvt_pk_bf16_f32 v167, v142, v143\n\t"
  "v_mfma_f32_16x16x32_bf16 v[28:31], a[124:127], v[176:179], v[28:31]\n\t"
  "v_exp_f32_e64 v144, v144\n\t"
  "v_mfma_f32_16x16x32_bf16 v[32:35], a[96:99], v[180:183], v[32:35]\n\t"
  "buffer_load_dwordx4 a[168:171], v200, %7, 0 offen offset:1024\n\t"
  "v_mad_u32_u16 v201, v207, s90, v227 op_sel:[1,0,0,0]\n\t"
  "v_mfma_f32_16x16x32_bf16 v[36:39], a[100:103], v[180:183], v[36:39]\n\t"
  "v_exp_f32_e64 v145, v145\n\t"
  "v_mfma_f32_16x16x32_bf16 v[40:43], a[104:107], v[180:183], v[40:43]\n\t"
  "ds_read_b128 a[72:75], v216 offset:41984\n\t"
  "v_cvt_pk_bf16_f32 v168, v144, v145\n\t"
  "v_mfma_f32_16x16x32_bf16 v[44:47], a[108:111], v[180:183], v[44:47]\n\t"
  "v_exp_f32_e64 v146, v146\n\t"
  "v_mfma_f32_16x16x32_bf16 v[48:51], a[112:115], v[180:183], v[48:51]\n\t"
  "buffer_load_dwordx4 v222, %7, s61 offen offset:2048 lds\n\t"
  "v_mfma_f32_16x16x32_bf16 v[52:55], a[116:119], v[180:183], v[52:55]\n\t"
  "v_exp_f32_e64 v147, v147\n\t"
  "v_mfma_f32_16x16x32_bf16 v[56:59], a[120:123], v[180:183], v[56:59]\n\t"
  "ds_read_b128 a[76:79], v216 offset:42496\n\t"
  "v_cvt_pk_bf16_f32 v169, v146, v147\n\t"
  "v_mfma_f32_16x16x32_bf16 v[60:63], a[124:127], v[180:183], v[60:63]\n\t"
  "v_exp_f32_e64 v148, v148\n\t"
  "v_mfma_f32_16x16x32_bf16 v[64:67], a[96:99], v[184:187], v[64:67]\n\t"
  "buffer_load_dwordx4 a[172:175], v201, %7, 0 offen offset:1024\n\t"
  "v_mad_u32_u16 v200, v208, s90, v227\n\t"
  "v_mfma_f32_16x16x32_bf16 v[68:71], a[100:103], v[184:187], v[68:71]\n\t"
  "v_exp_f32_e64 v149, v149\n\t"
  "v_mfma_f32_16x16x32_bf16 v[72:75], a[104:107], v[184:187], v[72:75]\n\t"
  "ds_read_b128 a[80:83], v217 offset:40960\n\t"
  "v_cvt_pk_bf16_f32 v170, v148, v149\n\t"
  "v_mfma_f32_16x16x32_bf16 v[76:79], a[108:111], v[184:187], v[76:79]\n\t"
  "v_exp_f32_e64 v150, v150\n\t"
  "v_mfma_f32_16x16x32_bf16 v[80:83], a[112:115], v[184:187], v[80:83]\n\t"
  "buffer_load_dwordx4 v223, %7, s61 offen offset:3072 lds\n\t"
  "v_mfma_f32_16x16x32_bf16 v[84:87], a[116:119], v[184:187], v[84:87]\n\t"
  "s_add_u32 s63, s56, 0x0\n\t"
  "v_exp_f32_e64 v151, v151\n\t"
  "v_mfma_f32_16x16x32_bf16 v[88:91], a[120:123], v[184:187], v[88:91]\n\t"
  "s_add_u32 m0, s57, 0x0\n\t"
  "ds_read_b128 a[84:87], v217 offset:41472\n\t"
  "v_cvt_pk_bf16_f32 v171, v150, v151\n\t"
  "v_mfma_f32_16x16x32_bf16 v[92:95], a[124:127], v[184:187], v[92:95]\n\t"
  "v_exp_f32_e64 v152, v152\n\t"
  "v_mfma_f32_16x16x32_bf16 v[96:99], a[96:99], v[188:191], v[96:99]\n\t"
  "ds_read_b128 a[92:95], v217 offset:42496\n\t"
  "ds_read_b128 v[202:205], v229 offset:384\n\t"
  "v_mfma_f32_16x16x32_bf16 v[100:103], a[100:103], v[188:191], v[100:103]\n\t"
  "v_exp_f32_e64 v153, v153\n\t"
  "v_mfma_f32_16x16x32_bf16 v[104:107], a[104:107], v[188:191], v[104:107]\n\t"
  "ds_read_b128 a[88:91], v217 offset:41984\n\t"
  "v_cvt_pk_bf16_f32 v172, v152, v153\n\t"
  "v_mfma_f32_16x16x32_bf16 v[108:111], a[108:111], v[188:191], v[108:111]\n\t"
  "v_exp_f32_e64 v154, v154\n\t"
  "v_mfma_f32_16x16x32_bf16 v[112:115], a[112:115], v[188:191], v[112:115]\n\t"
  "buffer_load_dwordx4 v224, %8, s63 offen offset:0 lds\n\t"
  "v_mfma_f32_16x16x32_bf16 v[116:119], a[116:119], v[188:191], v[116:119]\n\t"
  "v_exp_f32_e64 v155, v155\n\t"
  "v_mfma_f32_16x16x32_bf16 v[120:123], a[120:123], v[188:191], v[120:123]\n\t"
  "v_cvt_pk_bf16_f32 v173, v154, v155\n\t"
  "v_mfma_f32_16x16x32_bf16 v[124:127], a[124:127], v[188:191], v[124:127]\n\t"
  "s_waitcnt lgkmcnt(3)\n\t"
  "v_exp_f32_e32 v156, v156\n\t"
  "v_mfma_f32_16x16x32_bf16 v[128:131], a[64:67], a[0:3], 0\n\t"
  "ds_read_b64_tr_b16 a[96:97], v218 offset:32768\n\t"
  "ds_read_b64_tr_b16 a[98:99], v219 offset:32768\n\t"
  "v_mfma_f32_16x16x32_bf16 v[128:131], a[68:71], a[4:7], v[128:131]\n\t"
  "v_exp_f32_e64 v157, v157\n\t"
  "v_mfma_f32_16x16x32_bf16 v[128:131], a[72:75], a[8:11], v[128:131]\n\t"
  "ds_read_b64_tr_b16 a[100:101], v196 offset:32768\n\t"
  "v_cvt_pk_bf16_f32 v174, v156, v157\n\t"
  "v_mfma_f32_16x16x32_bf16 v[128:131], a[76:79], a[12:15], v[128:131]\n\t"
  "v_exp_f32_e64 v158, v158\n\t"
  "v_mfma_f32_16x16x32_bf16 v[132:135], a[80:83], a[0:3], 0\n\t"
  "buffer_load_dwordx4 v224, %8, s63 offen offset:1024 lds\n\t"
  "v_mfma_f32_16x16x32_bf16 v[132:135], a[84:87], a[4:7], v[132:135]\n\t"
  "s_waitcnt lgkmcnt(3)\n\t"
  "v_exp_f32_e32 v159, v159\n\t"
  "v_mfma_f32_16x16x32_bf16 v[132:135], a[88:91], a[8:11], v[132:135]\n\t"
  "ds_read_b64_tr_b16 a[102:103], v197 offset:32768\n\t"
  "v_cvt_pk_bf16_f32 v175, v158, v159\n\t"
  "v_mfma_f32_16x16x32_bf16 v[132:135], a[92:95], a[12:15], v[132:135]\n\t"
  "v_exp_f32_e64 v128, v128\n\t"
  "v_mfma_f32_16x16x32_bf16 v[136:139], a[64:67], a[16:19], 0\n\t"
  "ds_read_b64_tr_b16 a[104:105], v218 offset:33280\n\t"
  "ds_read_b64_tr_b16 a[106:107], v219 offset:33280\n\t"
  "v_mfma_f32_16x16x32_bf16 v[136:139], a[68:71], a[20:23], v[136:139]\n\t"
  "v_exp_f32_e64 v129, v129\n\t"
  "v_mfma_f32_16x16x32_bf16 v[136:139], a[72:75], a[24:27], v[136:139]\n\t"
  "ds_read_b64_tr_b16 a[108:109], v196 offset:33280\n\t"
  "v_cvt_pk_bf16_f32 v176, v128, v129\n\t"
  "v_mfma_f32_16x16x32_bf16 v[136:139], a[76:79], a[28:31], v[136:139]\n\t"
  "v_exp_f32_e64 v130, v130\n\t"
  "v_mfma_f32_16x16x32_bf16 v[140:143], a[80:83], a[16:19], 0\n\t"
  "buffer_load_dwordx4 v224, %8, s63 offen offset:2048 lds\n\t"
  "v_mfma_f32_16x16x32_bf16 v[140:143], a[84:87], a[20:23], v[140:143]\n\t"
  "v_exp_f32_e64 v131, v131\n\t"
  "v_mfma_f32_16x16x32_bf16 v[140:143], a[88:91], a[24:27], v[140:143]\n\t"
  "ds_read_b64_tr_b16 a[110:111], v197 offset:33280\n\t"
  "v_cvt_pk_bf16_f32 v177, v130, v131\n\t"
  "v_mfma_f32_16x16x32_bf16 v[140:143], a[92:95], a[28:31], v[140:143]\n\t"
  "v_exp_f32_e64 v132, v132\n\t"
  "v_mfma_f32_16x16x32_bf16 v[144:147], a[64:67], a[32:35], 0\n\t"
  "ds_read_b64_tr_b16 a[112:113], v218 offset:33792\n\t"
  "ds_read_b64_tr_b16 a[114:115], v219 offset:33792\n\t"
  "v_mfma_f32_16x16x32_bf16 v[144:147], a[68:71], a[36:39], v[144:147]\n\t"
  "v_exp_f32_e64 v133, v133\n\t"
  "v_mfma_f32_16x16x32_bf16 v[144:147], a[72:75], a[40:43], v[144:147]\n\t"
  "ds_read_b64_tr_b16 a[116:117], v196 offset:33792\n\t"
  "v_cvt_pk_bf16_f32 v178, v132, v133\n\t"
  "v_mfma_f32_16x16x32_bf16 v[144:147], a[76:79], a[44:47], v[144:147]\n\t"
  "v_exp_f32_e64 v134, v134\n\t"
  "v_mfma_f32_16x16x32_bf16 v[148:151], a[80:83], a[32:35], 0\n\t"
  "buffer_load_dwordx4 v224, %8, s63 offen offset:3072 lds\n\t"
  "v_mfma_f32_16x16x32_bf16 v[148:151], a[84:87], a[36:39], v[148:151]\n\t"
  "v_exp_f32_e64 v135, v135\n\t"
  "v_mfma_f32_16x16x32_bf16 v[148:151], a[88:91], a[40:43], v[148:151]\n\t"
  "ds_read_b64_tr_b16 a[118:119], v197 offset:33792\n\t"
  "v_cvt_pk_bf16_f32 v179, v134, v135\n\t"
  "v_mfma_f32_16x16x32_bf16 v[148:151], a[92:95], a[44:47], v[148:151]\n\t"
  "v_exp_f32_e64 v136, v136\n\t"
  "v_mfma_f32_16x16x32_bf16 v[152:155], a[64:67], a[48:51], 0\n\t"
  "ds_read_b64_tr_b16 a[120:121], v218 offset:34304\n\t"
  "ds_read_b64_tr_b16 a[122:123], v219 offset:34304\n\t"
  "v_mfma_f32_16x16x32_bf16 v[152:155], a[68:71], a[52:55], v[152:155]\n\t"
  "v_exp_f32_e64 v137, v137\n\t"
  "v_mfma_f32_16x16x32_bf16 v[152:155], a[72:75], a[56:59], v[152:155]\n\t"
  "ds_read_b64_tr_b16 a[124:125], v196 offset:34304\n\t"
  "v_cvt_pk_bf16_f32 v180, v136, v137\n\t"
  "v_mfma_f32_16x16x32_bf16 v[152:155], a[76:79], a[60:63], v[152:155]\n\t"
  "v_exp_f32_e64 v138, v138\n\t"
  "v_mfma_f32_16x16x32_bf16 v[156:159], a[80:83], a[48:51], 0\n\t"
  "s_waitcnt vmcnt(20)\n\t"
  "s_barrier\n\t"
  "v_mfma_f32_16x16x32_bf16 v[156:159], a[84:87], a[52:55], v[156:159]\n\t"
  "v_exp_f32_e64 v139, v139\n\t"
  "v_mfma_f32_16x16x32_bf16 v[156:159], a[88:91], a[56:59], v[156:159]\n\t"
  "ds_read_b64_tr_b16 a[126:127], v197 offset:34304\n\t"
  "v_cvt_pk_bf16_f32 v181, v138, v139\n\t"
  "v_mfma_f32_16x16x32_bf16 v[156:159], a[92:95], a[60:63], v[156:159]\n\t"
  "s_waitcnt lgkmcnt(2)\n\t"
  "v_exp_f32_e32 v140, v140\n\t"
  "v_mfma_f32_16x16x32_bf16 v[0:3], a[96:99], v[160:163], v[0:3]\n\t"
  "s_add_u32 m0, s54, 0x4000\n\t"
  "buffer_load_dwordx4 a[176:179], v200, %7, 0 offen offset:1024\n\t"
  "v_mad_u32_u16 v201, v208, s90, v227 op_sel:[1,0,0,0]\n\t"
  "v_mfma_f32_16x16x32_bf16 v[4:7], a[100:103], v[160:163], v[4:7]\n\t"
  "s_add_u32 s61, s61, 0x4000\n\t"
  "v_exp_f32_e64 v141, v141\n\t"
  "v_mfma_f32_16x16x32_bf16 v[8:11], a[104:107], v[160:163], v[8:11]\n\t"
  "ds_read_b128 a[64:67], v216 offset:49152\n\t"
  "v_cvt_pk_bf16_f32 v182, v140, v141\n\t"
  "v_mfma_f32_16x16x32_bf16 v[12:15], a[108:111], v[160:163], v[12:15]\n\t"
  "v_exp_f32_e64 v142, v142\n\t"
  "v_mfma_f32_16x16x32_bf16 v[16:19], a[112:115], v[160:163], v[16:19]\n\t"
  "buffer_load_dwordx4 a[180:183], v201, %7, 0 offen offset:1024\n\t"
  "v_mad_u32_u16 v200, v209, s90, v227\n\t"
  "v_mfma_f32_16x16x32_bf16 v[20:23], a[116:119], v[160:163], v[20:23]\n\t"
  "s_waitcnt lgkmcnt(1)\n\t"
  "v_exp_f32_e32 v143, v143\n\t"
  "v_mfma_f32_16x16x32_bf16 v[24:27], a[120:123], v[160:163], v[24:27]\n\t"
  "ds_read_b128 a[68:71], v216 offset:49664\n\t"
  "v_cvt_pk_bf16_f32 v183, v142, v143\n\t"
  "v_mfma_f32_16x16x32_bf16 v[28:31], a[124:127], v[160:163], v[28:31]\n\t"
  "v_exp_f32_e64 v144, v144\n\t"
  "v_mfma_f32_16x16x32_bf16 v[32:35], a[96:99], v[164:167], v[32:35]\n\t"
  "buffer_load_dwordx4 a[184:187], v200, %7, 0 offen offset:1024\n\t"
  "v_mad_u32_u16 v201, v209, s90, v227 op_sel:[1,0,0,0]\n\t"
  "v_mfma_f32_16x16x32_bf16 v[36:39], a[100:103], v[164:167], v[36:39]\n\t"
  "v_exp_f32_e64 v145, v145\n\t"
  "v_mfma_f32_16x16x32_bf16 v[40:43], a[104:107], v[164:167], v[40:43]\n\t"
  "ds_read_b128 a[72:75], v216 offset:50176\n\t"
  "v_cvt_pk_bf16_f32 v184, v144, v145\n\t"
  "v_mfma_f32_16x16x32_bf16 v[44:47], a[108:111], v[164:167], v[44:47]\n\t"
  "v_exp_f32_e64 v146, v146\n\t"
  "v_mfma_f32_16x16x32_bf16 v[48:51], a[112:115], v[164:167], v[48:51]\n\t"
  "buffer_load_dwordx4 v220, %7, s61 offen offset:0 lds\n\t"
  "v_mfma_f32_16x16x32_bf16 v[52:55], a[116:119], v[164:167], v[52:55]\n\t"
  "v_exp_f32_e64 v147, v147\n\t"
  "v_mfma_f32_16x16x32_bf16 v[56:59], a[120:123], v[164:167], v[56:59]\n\t"
  "ds_read_b128 a[76:79], v216 offset:50688\n\t"
  "v_cvt_pk_bf16_f32 v185, v146, v147\n\t"
  "v_mfma_f32_16x16x32_bf16 v[60:63], a[124:127], v[164:167], v[60:63]\n\t"
  "v_exp_f32_e64 v148, v148\n\t"
  "v_mfma_f32_16x16x32_bf16 v[64:67], a[96:99], v[168:171], v[64:67]\n\t"
  "buffer_load_dwordx4 a[188:191], v201, %7, 0 offen offset:1024\n\t"
  "v_mad_u32_u16 v200, v210, s90, v227\n\t"
  "v_mfma_f32_16x16x32_bf16 v[68:71], a[100:103], v[168:171], v[68:71]\n\t"
  "v_exp_f32_e64 v149, v149\n\t"
  "v_mfma_f32_16x16x32_bf16 v[72:75], a[104:107], v[168:171], v[72:75]\n\t"
  "ds_read_b128 a[80:83], v217 offset:49152\n\t"
  "v_cvt_pk_bf16_f32 v186, v148, v149\n\t"
  "v_mfma_f32_16x16x32_bf16 v[76:79], a[108:111], v[168:171], v[76:79]\n\t"
  "v_exp_f32_e64 v150, v150\n\t"
  "v_mfma_f32_16x16x32_bf16 v[80:83], a[112:115], v[168:171], v[80:83]\n\t"
  "buffer_load_dwordx4 v221, %7, s61 offen offset:1024 lds\n\t"
  "v_mfma_f32_16x16x32_bf16 v[84:87], a[116:119], v[168:171], v[84:87]\n\t"
  "v_exp_f32_e64 v151, v151\n\t"
  "v_mfma_f32_16x16x32_bf16 v[88:91], a[120:123], v[168:171], v[88:91]\n\t"
  "ds_read_b128 a[84:87], v217 offset:49664\n\t"
  "v_cvt_pk_bf16_f32 v187, v150, v151\n\t"
  "v_mfma_f32_16x16x32_bf16 v[92:95], a[124:127], v[168:171], v[92:95]\n\t"
  "v_exp_f32_e64 v152, v152\n\t"
  "v_mfma_f32_16x16x32_bf16 v[96:99], a[96:99], v[172:175], v[96:99]\n\t"
  "ds_read_b128 a[92:95], v217 offset:50688\n\t"
  "ds_read_b128 v[206:209], v229 offset:512\n\t"
  "v_mfma_f32_16x16x32_bf16 v[100:103], a[100:103], v[172:175], v[100:103]\n\t"
  "v_exp_f32_e64 v153, v153\n\t"
  "v_mfma_f32_16x16x32_bf16 v[104:107], a[104:107], v[172:175], v[104:107]\n\t"
  "ds_read_b128 a[88:91], v217 offset:50176\n\t"
  "v_cvt_pk_bf16_f32 v188, v152, v153\n\t"
  "v_mfma_f32_16x16x32_bf16 v[108:111], a[108:111], v[172:175], v[108:111]\n\t"
  "v_exp_f32_e64 v154, v154\n\t"
  "v_mfma_f32_16x16x32_bf16 v[112:115], a[112:115], v[172:175], v[112:115]\n\t"
  "buffer_load_dwordx4 a[192:195], v200, %7, 0 offen offset:1024\n\t"
  "v_mad_u32_u16 v201, v210, s90, v227 op_sel:[1,0,0,0]\n\t"
  "v_mfma_f32_16x16x32_bf16 v[116:119], a[116:119], v[172:175], v[116:119]\n\t"
  "v_exp_f32_e64 v155, v155\n\t"
  "v_mfma_f32_16x16x32_bf16 v[120:123], a[120:123], v[172:175], v[120:123]\n\t"
  "v_cvt_pk_bf16_f32 v189, v154, v155\n\t"
  "v_mfma_f32_16x16x32_bf16 v[124:127], a[124:127], v[172:175], v[124:127]\n\t"
  "s_waitcnt lgkmcnt(3)\n\t"
  "v_exp_f32_e32 v156, v156\n\t"
  "v_mfma_f32_16x16x32_bf16 v[128:131], a[64:67], a[0:3], 0\n\t"
  "ds_read_b64_tr_b16 a[96:97], v218 offset:40960\n\t"
  "ds_read_b64_tr_b16 a[98:99], v219 offset:40960\n\t"
  "v_mfma_f32_16x16x32_bf16 v[128:131], a[68:71], a[4:7], v[128:131]\n\t"
  "v_exp_f32_e64 v157, v157\n\t"
  "v_mfma_f32_16x16x32_bf16 v[128:131], a[72:75], a[8:11], v[128:131]\n\t"
  "ds_read_b64_tr_b16 a[100:101], v196 offset:40960\n\t"
  "v_cvt_pk_bf16_f32 v190, v156, v157\n\t"
  "v_mfma_f32_16x16x32_bf16 v[128:131], a[76:79], a[12:15], v[128:131]\n\t"
  "v_exp_f32_e64 v158, v158\n\t"
  "v_mfma_f32_16x16x32_bf16 v[132:135], a[80:83], a[0:3], 0\n\t"
  "buffer_load_dwordx4 a[196:199], v201, %7, 0 offen offset:1024\n\t"
  "v_mad_u32_u16 v200, v211, s90, v227\n\t"
  "v_mfma_f32_16x16x32_bf16 v[132:135], a[84:87], a[4:7], v[132:135]\n\t"
  "s_waitcnt lgkmcnt(3)\n\t"
  "v_exp_f32_e32 v159, v159\n\t"
  "v_mfma_f32_16x16x32_bf16 v[132:135], a[88:91], a[8:11], v[132:135]\n\t"
  "ds_read_b64_tr_b16 a[102:103], v197 offset:40960\n\t"
  "v_cvt_pk_bf16_f32 v191, v158, v159\n\t"
  "v_mfma_f32_16x16x32_bf16 v[132:135], a[92:95], a[12:15], v[132:135]\n\t"
  "v_exp_f32_e64 v128, v128\n\t"
  "v_mfma_f32_16x16x32_bf16 v[136:139], a[64:67], a[16:19], 0\n\t"
  "ds_read_b64_tr_b16 a[104:105], v218 offset:41472\n\t"
  "ds_read_b64_tr_b16 a[106:107], v219 offset:41472\n\t"
  "v_mfma_f32_16x16x32_bf16 v[136:139], a[68:71], a[20:23], v[136:139]\n\t"
  "v_exp_f32_e64 v129, v129\n\t"
  "v_mfma_f32_16x16x32_bf16 v[136:139], a[72:75], a[24:27], v[136:139]\n\t"
  "ds_read_b64_tr_b16 a[108:109], v196 offset:41472\n\t"
  "v_cvt_pk_bf16_f32 v160, v128, v129\n\t"
  "v_mfma_f32_16x16x32_bf16 v[136:139], a[76:79], a[28:31], v[136:139]\n\t"
  "v_exp_f32_e64 v130, v130\n\t"
  "v_mfma_f32_16x16x32_bf16 v[140:143], a[80:83], a[16:19], 0\n\t"
  "buffer_load_dwordx4 a[200:203], v200, %7, 0 offen offset:1024\n\t"
  "v_mad_u32_u16 v201, v211, s90, v227 op_sel:[1,0,0,0]\n\t"
  "v_mfma_f32_16x16x32_bf16 v[140:143], a[84:87], a[20:23], v[140:143]\n\t"
  "v_exp_f32_e64 v131, v131\n\t"
  "v_mfma_f32_16x16x32_bf16 v[140:143], a[88:91], a[24:27], v[140:143]\n\t"
  "ds_read_b64_tr_b16 a[110:111], v197 offset:41472\n\t"
  "v_cvt_pk_bf16_f32 v161, v130, v131\n\t"
  "v_mfma_f32_16x16x32_bf16 v[140:143], a[92:95], a[28:31], v[140:143]\n\t"
  "v_exp_f32_e64 v132, v132\n\t"
  "v_mfma_f32_16x16x32_bf16 v[144:147], a[64:67], a[32:35], 0\n\t"
  "ds_read_b64_tr_b16 a[112:113], v218 offset:41984\n\t"
  "ds_read_b64_tr_b16 a[114:115], v219 offset:41984\n\t"
  "v_mfma_f32_16x16x32_bf16 v[144:147], a[68:71], a[36:39], v[144:147]\n\t"
  "v_exp_f32_e64 v133, v133\n\t"
  "v_mfma_f32_16x16x32_bf16 v[144:147], a[72:75], a[40:43], v[144:147]\n\t"
  "ds_read_b64_tr_b16 a[116:117], v196 offset:41984\n\t"
  "v_cvt_pk_bf16_f32 v162, v132, v133\n\t"
  "v_mfma_f32_16x16x32_bf16 v[144:147], a[76:79], a[44:47], v[144:147]\n\t"
  "v_exp_f32_e64 v134, v134\n\t"
  "v_mfma_f32_16x16x32_bf16 v[148:151], a[80:83], a[32:35], 0\n\t"
  "buffer_load_dwordx4 a[204:207], v201, %7, 0 offen offset:1024\n\t"
  "v_mad_u32_u16 v200, v212, s90, v227\n\t"
  "v_mfma_f32_16x16x32_bf16 v[148:151], a[84:87], a[36:39], v[148:151]\n\t"
  "v_exp_f32_e64 v135, v135\n\t"
  "v_mfma_f32_16x16x32_bf16 v[148:151], a[88:91], a[40:43], v[148:151]\n\t"
  "ds_read_b64_tr_b16 a[118:119], v197 offset:41984\n\t"
  "v_cvt_pk_bf16_f32 v163, v134, v135\n\t"
  "v_mfma_f32_16x16x32_bf16 v[148:151], a[92:95], a[44:47], v[148:151]\n\t"
  "v_exp_f32_e64 v136, v136\n\t"
  "v_mfma_f32_16x16x32_bf16 v[152:155], a[64:67], a[48:51], 0\n\t"
  "ds_read_b64_tr_b16 a[120:121], v218 offset:42496\n\t"
  "ds_read_b64_tr_b16 a[122:123], v219 offset:42496\n\t"
  "v_mfma_f32_16x16x32_bf16 v[152:155], a[68:71], a[52:55], v[152:155]\n\t"
  "v_exp_f32_e64 v137, v137\n\t"
  "v_mfma_f32_16x16x32_bf16 v[152:155], a[72:75], a[56:59], v[152:155]\n\t"
  "ds_read_b64_tr_b16 a[124:125], v196 offset:42496\n\t"
  "v_cvt_pk_bf16_f32 v164, v136, v137\n\t"
  "v_mfma_f32_16x16x32_bf16 v[152:155], a[76:79], a[60:63], v[152:155]\n\t"
  "v_exp_f32_e64 v138, v138\n\t"
  "v_mfma_f32_16x16x32_bf16 v[156:159], a[80:83], a[48:51], 0\n\t"
  "buffer_load_dwordx4 a[208:211], v200, %7, 0 offen offset:1024\n\t"
  "v_mad_u32_u16 v201, v212, s90, v227 op_sel:[1,0,0,0]\n\t"
  "v_mfma_f32_16x16x32_bf16 v[156:159], a[84:87], a[52:55], v[156:159]\n\t"
  "v_exp_f32_e64 v139, v139\n\t"
  "v_mfma_f32_16x16x32_bf16 v[156:159], a[88:91], a[56:59], v[156:159]\n\t"
  "ds_read_b64_tr_b16 a[126:127], v197 offset:42496\n\t"
  "v_cvt_pk_bf16_f32 v165, v138, v139\n\t"
  "v_mfma_f32_16x16x32_bf16 v[156:159], a[92:95], a[60:63], v[156:159]\n\t"
  "s_waitcnt lgkmcnt(2)\n\t"
  "v_exp_f32_e32 v140, v140\n\t"
  "v_mfma_f32_16x16x32_bf16 v[0:3], a[96:99], v[176:179], v[0:3]\n\t"
  "buffer_load_dwordx4 a[212:215], v201, %7, 0 offen offset:1024\n\t"
  "v_mad_u32_u16 v200, v213, s90, v227\n\t"
  "v_mfma_f32_16x16x32_bf16 v[4:7], a[100:103], v[176:179], v[4:7]\n\t"
  "v_exp_f32_e64 v141, v141\n\t"
  "v_mfma_f32_16x16x32_bf16 v[8:11], a[104:107], v[176:179], v[8:11]\n\t"
  "ds_read_b128 a[64:67], v216 offset:57344\n\t"
  "v_cvt_pk_bf16_f32 v166, v140, v141\n\t"
  "v_mfma_f32_16x16x32_bf16 v[12:15], a[108:111], v[176:179], v[12:15]\n\t"
  "v_exp_f32_e64 v142, v142\n\t"
  "v_mfma_f32_16x16x32_bf16 v[16:19], a[112:115], v[176:179], v[16:19]\n\t"
  "buffer_load_dwordx4 a[216:219], v200, %7, 0 offen offset:1024\n\t"
  "v_mad_u32_u16 v201, v213, s90, v227 op_sel:[1,0,0,0]\n\t"
  "v_mfma_f32_16x16x32_bf16 v[20:23], a[116:119], v[176:179], v[20:23]\n\t"
  "s_waitcnt lgkmcnt(1)\n\t"
  "v_exp_f32_e32 v143, v143\n\t"
  "v_mfma_f32_16x16x32_bf16 v[24:27], a[120:123], v[176:179], v[24:27]\n\t"
  "ds_read_b128 a[68:71], v216 offset:57856\n\t"
  "v_cvt_pk_bf16_f32 v167, v142, v143\n\t"
  "v_mfma_f32_16x16x32_bf16 v[28:31], a[124:127], v[176:179], v[28:31]\n\t"
  "v_exp_f32_e64 v144, v144\n\t"
  "v_mfma_f32_16x16x32_bf16 v[32:35], a[96:99], v[180:183], v[32:35]\n\t"
  "buffer_load_dwordx4 a[220:223], v201, %7, 0 offen offset:1024\n\t"
  "v_mad_u32_u16 v200, v202, s90, v227\n\t"
  "v_mfma_f32_16x16x32_bf16 v[36:39], a[100:103], v[180:183], v[36:39]\n\t"
  "v_exp_f32_e64 v145, v145\n\t"
  "v_mfma_f32_16x16x32_bf16 v[40:43], a[104:107], v[180:183], v[40:43]\n\t"
  "ds_read_b128 a[72:75], v216 offset:58368\n\t"
  "v_cvt_pk_bf16_f32 v168, v144, v145\n\t"
  "v_mfma_f32_16x16x32_bf16 v[44:47], a[108:111], v[180:183], v[44:47]\n\t"
  "v_exp_f32_e64 v146, v146\n\t"
  "v_mfma_f32_16x16x32_bf16 v[48:51], a[112:115], v[180:183], v[48:51]\n\t"
  "buffer_load_dwordx4 v222, %7, s61 offen offset:2048 lds\n\t"
  "v_mfma_f32_16x16x32_bf16 v[52:55], a[116:119], v[180:183], v[52:55]\n\t"
  "v_exp_f32_e64 v147, v147\n\t"
  "v_mfma_f32_16x16x32_bf16 v[56:59], a[120:123], v[180:183], v[56:59]\n\t"
  "ds_read_b128 a[76:79], v216 offset:58880\n\t"
  "v_cvt_pk_bf16_f32 v169, v146, v147\n\t"
  "v_mfma_f32_16x16x32_bf16 v[60:63], a[124:127], v[180:183], v[60:63]\n\t"
  "v_exp_f32_e64 v148, v148\n\t"
  "v_mfma_f32_16x16x32_bf16 v[64:67], a[96:99], v[184:187], v[64:67]\n\t"
  "buffer_load_dwordx4 a[224:227], v200, %7, 0 offen offset:1024\n\t"
  "v_mad_u32_u16 v201, v202, s90, v227 op_sel:[1,0,0,0]\n\t"
  "v_mfma_f32_16x16x32_bf16 v[68:71], a[100:103], v[184:187], v[68:71]\n\t"
  "v_exp_f32_e64 v149, v149\n\t"
  "v_mfma_f32_16x16x32_bf16 v[72:75], a[104:107], v[184:187], v[72:75]\n\t"
  "ds_read_b128 a[80:83], v217 offset:57344\n\t"
  "v_cvt_pk_bf16_f32 v170, v148, v149\n\t"
  "v_mfma_f32_16x16x32_bf16 v[76:79], a[108:111], v[184:187], v[76:79]\n\t"
  "v_exp_f32_e64 v150, v150\n\t"
  "v_mfma_f32_16x16x32_bf16 v[80:83], a[112:115], v[184:187], v[80:83]\n\t"
  "buffer_load_dwordx4 v223, %7, s61 offen offset:3072 lds\n\t"
  "v_mfma_f32_16x16x32_bf16 v[84:87], a[116:119], v[184:187], v[84:87]\n\t"
  "s_add_u32 s63, s56, 0x1000\n\t"
  "v_exp_f32_e64 v151, v151\n\t"
  "v_mfma_f32_16x16x32_bf16 v[88:91], a[120:123], v[184:187], v[88:91]\n\t"
  "s_add_u32 m0, s57, 0x1000\n\t"
  "ds_read_b128 a[84:87], v217 offset:57856\n\t"
  "v_cvt_pk_bf16_f32 v171, v150, v151\n\t"
  "v_mfma_f32_16x16x32_bf16 v[92:95], a[124:127], v[184:187], v[92:95]\n\t"
  "v_exp_f32_e64 v152, v152\n\t"
  "v_mfma_f32_16x16x32_bf16 v[96:99], a[96:99], v[188:191], v[96:99]\n\t"
  "ds_read_b128 a[92:95], v217 offset:58880\n\t"
  "ds_read_b128 v[210:213], v229 offset:640\n\t"
  "v_mfma_f32_16x16x32_bf16 v[100:103], a[100:103], v[188:191], v[100:103]\n\t"
  "v_exp_f32_e64 v153, v153\n\t"
  "v_mfma_f32_16x16x32_bf16 v[104:107], a[104:107], v[188:191], v[104:107]\n\t"
  "ds_read_b128 a[88:91], v217 offset:58368\n\t"
  "v_cvt_pk_bf16_f32 v172, v152, v153\n\t"
  "v_mfma_f32_16x16x32_bf16 v[108:111], a[108:111], v[188:191], v[108:111]\n\t"
  "v_exp_f32_e64 v154, v154\n\t"
  "v_mfma_f32_16x16x32_bf16 v[112:115], a[112:115], v[188:191], v[112:115]\n\t"
  "buffer_load_dwordx4 v224, %8, s63 offen offset:0 lds\n\t"
  "v_mfma_f32_16x16x32_bf16 v[116:119], a[116:119], v[188:191], v[116:119]\n\t"
  "v_exp_f32_e64 v155, v155\n\t"
  "v_mfma_f32_16x16x32_bf16 v[120:123], a[120:123], v[188:191], v[120:123]\n\t"
  "v_cvt_pk_bf16_f32 v173, v154, v155\n\t"
  "v_mfma_f32_16x16x32_bf16 v[124:127], a[124:127], v[188:191], v[124:127]\n\t"
  "s_waitcnt lgkmcnt(3)\n\t"
  "v_exp_f32_e32 v156, v156\n\t"
  "v_mfma_f32_16x16x32_bf16 v[128:131], a[64:67], a[0:3], 0\n\t"
  "ds_read_b64_tr_b16 a[96:97], v218 offset:49152\n\t"
  "ds_read_b64_tr_b16 a[98:99], v219 offset:49152\n\t"
  "v_mfma_f32_16x16x32_bf16 v[128:131], a[68:71], a[4:7], v[128:131]\n\t"
  "v_exp_f32_e64 v157, v157\n\t"
  "v_mfma_f32_16x16x32_bf16 v[128:131], a[72:75], a[8:11], v[128:131]\n\t"
  "ds_read_b64_tr_b16 a[100:101], v196 offset:49152\n\t"
  "v_cvt_pk_bf16_f32 v174, v156, v157\n\t"
  "v_mfma_f32_16x16x32_bf16 v[128:131], a[76:79], a[12:15], v[128:131]\n\t"
  "v_exp_f32_e64 v158, v158\n\t"
  "v_mfma_f32_16x16x32_bf16 v[132:135], a[80:83], a[0:3], 0\n\t"
  "buffer_load_dwordx4 v224, %8, s63 offen offset:1024 lds\n\t"
  "v_mfma_f32_16x16x32_bf16 v[132:135], a[84:87], a[4:7], v[132:135]\n\t"
  "s_waitcnt lgkmcnt(3)\n\t"
  "v_exp_f32_e32 v159, v159\n\t"
  "v_mfma_f32_16x16x32_bf16 v[132:135], a[88:91], a[8:11], v[132:135]\n\t"
  "ds_read_b64_tr_b16 a[102:103], v197 offset:49152\n\t"
  "v_cvt_pk_bf16_f32 v175, v158, v159\n\t"
  "v_mfma_f32_16x16x32_bf16 v[132:135], a[92:95], a[12:15], v[132:135]\n\t"
  "v_exp_f32_e64 v128, v128\n\t"
  "v_mfma_f32_16x16x32_bf16 v[136:139], a[64:67], a[16:19], 0\n\t"
  "ds_read_b64_tr_b16 a[104:105], v218 offset:49664\n\t"
  "ds_read_b64_tr_b16 a[106:107], v219 offset:49664\n\t"
  "v_mfma_f32_16x16x32_bf16 v[136:139], a[68:71], a[20:23], v[136:139]\n\t"
  "v_exp_f32_e64 v129, v129\n\t"
  "v_mfma_f32_16x16x32_bf16 v[136:139], a[72:75], a[24:27], v[136:139]\n\t"
  "ds_read_b64_tr_b16 a[108:109], v196 offset:49664\n\t"
  "v_cvt_pk_bf16_f32 v176, v128, v129\n\t"
  "v_mfma_f32_16x16x32_bf16 v[136:139], a[76:79], a[28:31], v[136:139]\n\t"
  "v_exp_f32_e64 v130, v130\n\t"
  "v_mfma_f32_16x16x32_bf16 v[140:143], a[80:83], a[16:19], 0\n\t"
  "buffer_load_dwordx4 v224, %8, s63 offen offset:2048 lds\n\t"
  "v_mfma_f32_16x16x32_bf16 v[140:143], a[84:87], a[20:23], v[140:143]\n\t"
  "v_exp_f32_e64 v131, v131\n\t"
  "v_mfma_f32_16x16x32_bf16 v[140:143], a[88:91], a[24:27], v[140:143]\n\t"
  "ds_read_b64_tr_b16 a[110:111], v197 offset:49664\n\t"
  "v_cvt_pk_bf16_f32 v177, v130, v131\n\t"
  "v_mfma_f32_16x16x32_bf16 v[140:143], a[92:95], a[28:31], v[140:143]\n\t"
  "v_exp_f32_e64 v132, v132\n\t"
  "v_mfma_f32_16x16x32_bf16 v[144:147], a[64:67], a[32:35], 0\n\t"
  "ds_read_b64_tr_b16 a[112:113], v218 offset:50176\n\t"
  "ds_read_b64_tr_b16 a[114:115], v219 offset:50176\n\t"
  "v_mfma_f32_16x16x32_bf16 v[144:147], a[68:71], a[36:39], v[144:147]\n\t"
  "v_exp_f32_e64 v133, v133\n\t"
  "v_mfma_f32_16x16x32_bf16 v[144:147], a[72:75], a[40:43], v[144:147]\n\t"
  "ds_read_b64_tr_b16 a[116:117], v196 offset:50176\n\t"
  "v_cvt_pk_bf16_f32 v178, v132, v133\n\t"
  "v_mfma_f32_16x16x32_bf16 v[144:147], a[76:79], a[44:47], v[144:147]\n\t"
  "v_exp_f32_e64 v134, v134\n\t"
  "v_mfma_f32_16x16x32_bf16 v[148:151], a[80:83], a[32:35], 0\n\t"
  "buffer_load_dwordx4 v224, %8, s63 offen offset:3072 lds\n\t"
  "v_mfma_f32_16x16x32_bf16 v[148:151], a[84:87], a[36:39], v[148:151]\n\t"
  "v_exp_f32_e64 v135, v135\n\t"
  "v_mfma_f32_16x16x32_bf16 v[148:151], a[88:91], a[40:43], v[148:151]\n\t"
  "ds_read_b64_tr_b16 a[118:119], v197 offset:50176\n\t"
  "v_cvt_pk_bf16_f32 v179, v134, v135\n\t"
  "v_mfma_f32_16x16x32_bf16 v[148:151], a[92:95], a[44:47], v[148:151]\n\t"
  "v_exp_f32_e64 v136, v136\n\t"
  "v_mfma_f32_16x16x32_bf16 v[152:155], a[64:67], a[48:51], 0\n\t"
  "ds_read_b64_tr_b16 a[120:121], v218 offset:50688\n\t"
  "ds_read_b64_tr_b16 a[122:123], v219 offset:50688\n\t"
  "v_mfma_f32_16x16x32_bf16 v[152:155], a[68:71], a[52:55], v[152:155]\n\t"
  "v_exp_f32_e64 v137, v137\n\t"
  "v_mfma_f32_16x16x32_bf16 v[152:155], a[72:75], a[56:59], v[152:155]\n\t"
  "ds_read_b64_tr_b16 a[124:125], v196 offset:50688\n\t"
  "v_cvt_pk_bf16_f32 v180, v136, v137\n\t"
  "v_mfma_f32_16x16x32_bf16 v[152:155], a[76:79], a[60:63], v[152:155]\n\t"
  "v_exp_f32_e64 v138, v138\n\t"
  "v_mfma_f32_16x16x32_bf16 v[156:159], a[80:83], a[48:51], 0\n\t"
  "s_waitcnt vmcnt(25)\n\t"
  "s_barrier\n\t"
  "v_mfma_f32_16x16x32_bf16 v[156:159], a[84:87], a[52:55], v[156:159]\n\t"
  "v_exp_f32_e64 v139, v139\n\t"
  "v_mfma_f32_16x16x32_bf16 v[156:159], a[88:91], a[56:59], v[156:159]\n\t"
  "ds_read_b64_tr_b16 a[126:127], v197 offset:50688\n\t"
  "v_cvt_pk_bf16_f32 v181, v138, v139\n\t"
  "v_mfma_f32_16x16x32_bf16 v[156:159], a[92:95], a[60:63], v[156:159]\n\t"
  "s_waitcnt lgkmcnt(2)\n\t"
  "v_exp_f32_e32 v140, v140\n\t"
  "v_mfma_f32_16x16x32_bf16 v[0:3], a[96:99], v[160:163], v[0:3]\n\t"
  "s_add_u32 m0, s54, 0x8000\n\t"
  "buffer_load_dwordx4 a[228:231], v201, %7, 0 offen offset:1024\n\t"
  "v_mad_u32_u16 v200, v203, s90, v227\n\t"
  "v_mfma_f32_16x16x32_bf16 v[4:7], a[100:103], v[160:163], v[4:7]\n\t"
  "s_add_u32 s61, s61, 0x4000\n\t"
  "v_exp_f32_e64 v141, v141\n\t"
  "v_mfma_f32_16x16x32_bf16 v[8:11], a[104:107], v[160:163], v[8:11]\n\t"
  "ds_read_b128 a[64:67], v216 offset:0\n\t"
  "v_cvt_pk_bf16_f32 v182, v140, v141\n\t"
  "v_mfma_f32_16x16x32_bf16 v[12:15], a[108:111], v[160:163], v[12:15]\n\t"
  "v_exp_f32_e64 v142, v142\n\t"
  "v_mfma_f32_16x16x32_bf16 v[16:19], a[112:115], v[160:163], v[16:19]\n\t"
  "buffer_load_dwordx4 a[232:235], v200, %7, 0 offen offset:1024\n\t"
  "v_mad_u32_u16 v201, v203, s90, v227 op_sel:[1,0,0,0]\n\t"
  "v_mfma_f32_16x16x32_bf16 v[20:23], a[116:119], v[160:163], v[20:23]\n\t"
  "s_waitcnt lgkmcnt(1)\n\t"
  "v_exp_f32_e32 v143, v143\n\t"
  "v_mfma_f32_16x16x32_bf16 v[24:27], a[120:123], v[160:163], v[24:27]\n\t"
  "ds_read_b128 a[68:71], v216 offset:512\n\t"
  "v_cvt_pk_bf16_f32 v183, v142, v143\n\t"
  "v_mfma_f32_16x16x32_bf16 v[28:31], a[124:127], v[160:163], v[28:31]\n\t"
  "v_exp_f32_e64 v144, v144\n\t"
  "v_mfma_f32_16x16x32_bf16 v[32:35], a[96:99], v[164:167], v[32:35]\n\t"
  "buffer_load_dwordx4 a[236:239], v201, %7, 0 offen offset:1024\n\t"
  "v_mad_u32_u16 v200, v204, s90, v227\n\t"
  "v_mfma_f32_16x16x32_bf16 v[36:39], a[100:103], v[164:167], v[36:39]\n\t"
  "v_exp_f32_e64 v145, v145\n\t"
  "v_mfma_f32_16x16x32_bf16 v[40:43], a[104:107], v[164:167], v[40:43]\n\t"
  "ds_read_b128 a[72:75], v216 offset:1024\n\t"
  "v_cvt_pk_bf16_f32 v184, v144, v145\n\t"
  "v_mfma_f32_16x16x32_bf16 v[44:47], a[108:111], v[164:167], v[44:47]\n\t"
  "v_exp_f32_e64 v146, v146\n\t"
  "v_mfma_f32_16x16x32_bf16 v[48:51], a[112:115], v[164:167], v[48:51]\n\t"
  "buffer_load_dwordx4 v220, %7, s61 offen offset:0 lds\n\t"
  "v_mfma_f32_16x16x32_bf16 v[52:55], a[116:119], v[164:167], v[52:55]\n\t"
  "v_exp_f32_e64 v147, v147\n\t"
  "v_mfma_f32_16x16x32_bf16 v[56:59], a[120:123], v[164:167], v[56:59]\n\t"
  "ds_read_b128 a[76:79], v216 offset:1536\n\t"
  "v_cvt_pk_bf16_f32 v185, v146, v147\n\t"
  "v_mfma_f32_16x16x32_bf16 v[60:63], a[124:127], v[164:167], v[60:63]\n\t"
  "v_exp_f32_e64 v148, v148\n\t"
  "v_mfma_f32_16x16x32_bf16 v[64:67], a[96:99], v[168:171], v[64:67]\n\t"
  "buffer_load_dwordx4 a[240:243], v200, %7, 0 offen offset:1024\n\t"
  "v_mad_u32_u16 v201, v204, s90, v227 op_sel:[1,0,0,0]\n\t"
  "v_mfma_f32_16x16x32_bf16 v[68:71], a[100:103], v[168:171], v[68:71]\n\t"
  "v_exp_f32_e64 v149, v149\n\t"
  "v_mfma_f32_16x16x32_bf16 v[72:75], a[104:107], v[168:171], v[72:75]\n\t"
  "ds_read_b128 a[80:83], v217 offset:0\n\t"
  "v_cvt_pk_bf16_f32 v186, v148, v149\n\t"
  "v_mfma_f32_16x16x32_bf16 v[76:79], a[108:111], v[168:171], v[76:79]\n\t"
  "v_exp_f32_e64 v150, v150\n\t"
  "v_mfma_f32_16x16x32_bf16 v[80:83], a[112:115], v[168:171], v[80:83]\n\t"
  "buffer_load_dwordx4 v221, %7, s61 offen offset:1024 lds\n\t"
  "v_mfma_f32_16x16x32_bf16 v[84:87], a[116:119], v[168:171], v[84:87]\n\t"
  "v_exp_f32_e64 v151, v151\n\t"
  "v_mfma_f32_16x16x32_bf16 v[88:91], a[120:123], v[168:171], v[88:91]\n\t"
  "ds_read_b128 a[84:87], v217 offset:512\n\t"
  "v_cvt_pk_bf16_f32 v187, v150, v151\n\t"
  "v_mfma_f32_16x16x32_bf16 v[92:95], a[124:127], v[168:171], v[92:95]\n\t"
  "v_exp_f32_e64 v152, v152\n\t"
  "v_mfma_f32_16x16x32_bf16 v[96:99], a[96:99], v[172:175], v[96:99]\n\t"
  "ds_read_b128 a[92:95], v217 offset:1536\n\t"
  "v_mfma_f32_16x16x32_bf16 v[100:103], a[100:103], v[172:175], v[100:103]\n\t"
  "v_exp_f32_e64 v153, v153\n\t"
  "v_mfma_f32_16x16x32_bf16 v[104:107], a[104:107], v[172:175], v[104:107]\n\t"
  "ds_read_b128 a[88:91], v217 offset:1024\n\t"
  "v_cvt_pk_bf16_f32 v188, v152, v153\n\t"
  "v_mfma_f32_16x16x32_bf16 v[108:111], a[108:111], v[172:175], v[108:111]\n\t"
  "v_exp_f32_e64 v154, v154\n\t"
  "v_mfma_f32_16x16x32_bf16 v[112:115], a[112:115], v[172:175], v[112:115]\n\t"
  "buffer_load_dwordx4 a[244:247], v201, %7, 0 offen offset:1024\n\t"
  "v_mad_u32_u16 v200, v205, s90, v227\n\t"
  "v_mfma_f32_16x16x32_bf16 v[116:119], a[116:119], v[172:175], v[116:119]\n\t"
  "v_exp_f32_e64 v155, v155\n\t"
  "v_mfma_f32_16x16x32_bf16 v[120:123], a[120:123], v[172:175], v[120:123]\n\t"
  "v_cvt_pk_bf16_f32 v189, v154, v155\n\t"
  "v_mfma_f32_16x16x32_bf16 v[124:127], a[124:127], v[172:175], v[124:127]\n\t"
  "s_waitcnt lgkmcnt(2)\n\t"
  "v_exp_f32_e32 v156, v156\n\t"
  "v_mfma_f32_16x16x32_bf16 v[128:131], a[64:67], a[0:3], 0\n\t"
  "ds_read_b64_tr_b16 a[96:97], v218 offset:57344\n\t"
  "ds_read_b64_tr_b16 a[98:99], v219 offset:57344\n\t"
  "v_mfma_f32_16x16x32_bf16 v[128:131], a[68:71], a[4:7], v[128:131]\n\t"
  "v_exp_f32_e64 v157, v157\n\t"
  "v_mfma_f32_16x16x32_bf16 v[128:131], a[72:75], a[8:11], v[128:131]\n\t"
  "ds_read_b64_tr_b16 a[100:101], v196 offset:57344\n\t"
  "v_cvt_pk_bf16_f32 v190, v156, v157\n\t"
  "v_mfma_f32_16x16x32_bf16 v[128:131], a[76:79], a[12:15], v[128:131]\n\t"
  "v_exp_f32_e64 v158, v158\n\t"
  "v_mfma_f32_16x16x32_bf16 v[132:135], a[80:83], a[0:3], 0\n\t"
  "buffer_load_dwordx4 a[248:251], v200, %7, 0 offen offset:1024\n\t"
  "v_mad_u32_u16 v201, v205, s90, v227 op_sel:[1,0,0,0]\n\t"
  "v_mfma_f32_16x16x32_bf16 v[132:135], a[84:87], a[4:7], v[132:135]\n\t"
  "s_waitcnt lgkmcnt(3)\n\t"
  "v_exp_f32_e32 v159, v159\n\t"
  "v_mfma_f32_16x16x32_bf16 v[132:135], a[88:91], a[8:11], v[132:135]\n\t"
  "ds_read_b64_tr_b16 a[102:103], v197 offset:57344\n\t"
  "v_cvt_pk_bf16_f32 v191, v158, v159\n\t"
  "v_mfma_f32_16x16x32_bf16 v[132:135], a[92:95], a[12:15], v[132:135]\n\t"
  "v_exp_f32_e64 v128, v128\n\t"
  "v_mfma_f32_16x16x32_bf16 v[136:139], a[64:67], a[16:19], 0\n\t"
  "ds_read_b64_tr_b16 a[104:105], v218 offset:57856\n\t"
  "ds_read_b64_tr_b16 a[106:107], v219 offset:57856\n\t"
  "v_mfma_f32_16x16x32_bf16 v[136:139], a[68:71], a[20:23], v[136:139]\n\t"
  "v_exp_f32_e64 v129, v129\n\t"
  "v_mfma_f32_16x16x32_bf16 v[136:139], a[72:75], a[24:27], v[136:139]\n\t"
  "ds_read_b64_tr_b16 a[108:109], v196 offset:57856\n\t"
  "v_cvt_pk_bf16_f32 v160, v128, v129\n\t"
  "v_mfma_f32_16x16x32_bf16 v[136:139], a[76:79], a[28:31], v[136:139]\n\t"
  "v_exp_f32_e64 v130, v130\n\t"
  "v_mfma_f32_16x16x32_bf16 v[140:143], a[80:83], a[16:19], 0\n\t"
  "buffer_load_dwordx4 a[252:255], v201, %7, 0 offen offset:1024\n\t"
  "v_mad_u32_u16 v200, v206, s90, v227\n\t"
  "v_mfma_f32_16x16x32_bf16 v[140:143], a[84:87], a[20:23], v[140:143]\n\t"
  "v_exp_f32_e64 v131, v131\n\t"
  "v_mfma_f32_16x16x32_bf16 v[140:143], a[88:91], a[24:27], v[140:143]\n\t"
  "ds_read_b64_tr_b16 a[110:111], v197 offset:57856\n\t"
  "v_cvt_pk_bf16_f32 v161, v130, v131\n\t"
  "v_mfma_f32_16x16x32_bf16 v[140:143], a[92:95], a[28:31], v[140:143]\n\t"
  "s_waitcnt vmcnt(48)\n\t"
  "v_exp_f32_e32 v132, v132\n\t"
  "v_mfma_f32_16x16x32_bf16 v[240:243], v[232:235], a[128:131], 0\n\t"
  "buffer_load_dwordx4 a[128:131], v200, %7, 0 offen offset:1024\n\t"
  "v_mad_u32_u16 v201, v206, s90, v227 op_sel:[1,0,0,0]\n\t"
  "v_mfma_f32_16x16x32_bf16 v[144:147], a[64:67], a[32:35], 0\n\t"
  "ds_read_b64_tr_b16 a[112:113], v218 offset:58368\n\t"
  "ds_read_b64_tr_b16 a[114:115], v219 offset:58368\n\t"
  "v_mfma_f32_16x16x32_bf16 v[144:147], a[68:71], a[36:39], v[144:147]\n\t"
  "v_exp_f32_e64 v133, v133\n\t"
  "v_mfma_f32_16x16x32_bf16 v[144:147], a[72:75], a[40:43], v[144:147]\n\t"
  "ds_read_b64_tr_b16 a[116:117], v196 offset:58368\n\t"
  "v_cvt_pk_bf16_f32 v162, v132, v133\n\t"
  "v_mfma_f32_16x16x32_bf16 v[144:147], a[76:79], a[44:47], v[144:147]\n\t"
  "v_exp_f32_e64 v134, v134\n\t"
  "v_mfma_f32_16x16x32_bf16 v[148:151], a[80:83], a[32:35], 0\n\t"
  "ds_read_b128 v[202:205], v229 offset:768\n\t"
  "v_mfma_f32_16x16x32_bf16 v[148:151], a[84:87], a[36:39], v[148:151]\n\t"
  "v_exp_f32_e64 v135, v135\n\t"
  "v_mfma_f32_16x16x32_bf16 v[148:151], a[88:91], a[40:43], v[148:151]\n\t"
  "ds_read_b64_tr_b16 a[118:119], v197 offset:58368\n\t"
  "v_cvt_pk_bf16_f32 v163, v134, v135\n\t"
  "v_mfma_f32_16x16x32_bf16 v[148:151], a[92:95], a[44:47], v[148:151]\n\t"
  "v_exp_f32_e64 v136, v136\n\t"
  "v_mfma_f32_16x16x32_bf16 v[240:243], v[232:235], a[132:135], v[240:243]\n\t"
  "buffer_load_dwordx4 a[132:135], v201, %7, 0 offen offset:1024\n\t"
  "v_mad_u32_u16 v200, v207, s90, v227\n\t"
  "v_mfma_f32_16x16x32_bf16 v[152:155], a[64:67], a[48:51], 0\n\t"
  "ds_read_b64_tr_b16 a[120:121], v218 offset:58880\n\t"
  "ds_read_b64_tr_b16 a[122:123], v219 offset:58880\n\t"
  "v_mfma_f32_16x16x32_bf16 v[152:155], a[68:71], a[52:55], v[152:155]\n\t"
  "v_exp_f32_e64 v137, v137\n\t"
  "v_mfma_f32_16x16x32_bf16 v[152:155], a[72:75], a[56:59], v[152:155]\n\t"
  "ds_read_b64_tr_b16 a[124:125], v196 offset:58880\n\t"
  "v_cvt_pk_bf16_f32 v164, v136, v137\n\t"
  "v_mfma_f32_16x16x32_bf16 v[152:155], a[76:79], a[60:63], v[152:155]\n\t"
  "v_exp_f32_e64 v138, v138\n\t"
  "v_mfma_f32_16x16x32_bf16 v[156:159], a[80:83], a[48:51], 0\n\t"
  "v_mfma_f32_16x16x32_bf16 v[156:159], a[84:87], a[52:55], v[156:159]\n\t"
  "s_waitcnt vmcnt(46)\n\t"
  "v_exp_f32_e32 v139, v139\n\t"
  "v_mfma_f32_16x16x32_bf16 v[156:159], a[88:91], a[56:59], v[156:159]\n\t"
  "ds_read_b64_tr_b16 a[126:127], v197 offset:58880\n\t"
  "v_cvt_pk_bf16_f32 v165, v138, v139\n\t"
  "v_mfma_f32_16x16x32_bf16 v[156:159], a[92:95], a[60:63], v[156:159]\n\t"
  "s_waitcnt lgkmcnt(2)\n\t"
  "v_exp_f32_e32 v140, v140\n\t"
  "v_mfma_f32_16x16x32_bf16 v[240:243], v[232:235], a[136:139], v[240:243]\n\t"
  "buffer_load_dwordx4 a[136:139], v200, %7, 0 offen offset:1024\n\t"
  "v_mad_u32_u16 v201, v207, s90, v227 op_sel:[1,0,0,0]\n\t"
  "v_mfma_f32_16x16x32_bf16 v[0:3], a[96:99], v[176:179], v[0:3]\n\t"
  "v_mfma_f32_16x16x32_bf16 v[4:7], a[100:103], v[176:179], v[4:7]\n\t"
  "v_exp_f32_e64 v141, v141\n\t"
  "v_mfma_f32_16x16x32_bf16 v[8:11], a[104:107], v[176:179], v[8:11]\n\t"
  "ds_read_b128 a[64:67], v216 offset:8192\n\t"
  "v_cvt_pk_bf16_f32 v166, v140, v141\n\t"
  "v_mfma_f32_16x16x32_bf16 v[12:15], a[108:111], v[176:179], v[12:15]\n\t"
  "v_exp_f32_e64 v142, v142\n\t"
  "v_mfma_f32_16x16x32_bf16 v[16:19], a[112:115], v[176:179], v[16:19]\n\t"
  "v_mfma_f32_16x16x32_bf16 v[20:23], a[116:119], v[176:179], v[20:23]\n\t"
  "s_waitcnt lgkmcnt(1)\n\t"
  "v_exp_f32_e32 v143, v143\n\t"
  "v_mfma_f32_16x16x32_bf16 v[24:27], a[120:123], v[176:179], v[24:27]\n\t"
  "ds_read_b128 a[68:71], v216 offset:8704\n\t"
  "v_cvt_pk_bf16_f32 v167, v142, v143\n\t"
  "v_mfma_f32_16x16x32_bf16 v[28:31], a[124:127], v[176:179], v[28:31]\n\t"
  "v_exp_f32_e64 v144, v144\n\t"
  "v_mfma_f32_16x16x32_bf16 v[240:243], v[232:235], a[140:143], v[240:243]\n\t"
  "buffer_load_dwordx4 a[140:143], v201, %7, 0 offen offset:1024\n\t"
  "v_mad_u32_u16 v200, v208, s90, v227\n\t"
  "v_mfma_f32_16x16x32_bf16 v[32:35], a[96:99], v[180:183], v[32:35]\n\t"
  "v_mfma_f32_16x16x32_bf16 v[36:39], a[100:103], v[180:183], v[36:39]\n\t"
  "v_exp_f32_e64 v145, v145\n\t"
  "v_mfma_f32_16x16x32_bf16 v[40:43], a[104:107], v[180:183], v[40:43]\n\t"
  "ds_read_b128 a[72:75], v216 offset:9216\n\t"
  "v_cvt_pk_bf16_f32 v168, v144, v145\n\t"
  "v_mfma_f32_16x16x32_bf16 v[44:47], a[108:111], v[180:183], v[44:47]\n\t"
  "v_exp_f32_e64 v146, v146\n\t"
  "v_mfma_f32_16x16x32_bf16 v[48:51], a[112:115], v[180:183], v[48:51]\n\t"
  "buffer_load_dwordx4 v222, %7, s61 offen offset:2048 lds\n\t"
  "v_mfma_f32_16x16x32_bf16 v[52:55], a[116:119], v[180:183], v[52:55]\n\t"
  "v_exp_f32_e64 v147, v147\n\t"
  "v_mfma_f32_16x16x32_bf16 v[56:59], a[120:123], v[180:183], v[56:59]\n\t"
  "ds_read_b128 a[76:79], v216 offset:9728\n\t"
  "v_cvt_pk_bf16_f32 v169, v146, v147\n\t"
  "v_mfma_f32_16x16x32_bf16 v[60:63], a[124:127], v[180:183], v[60:63]\n\t"
  "s_waitcnt vmcnt(47)\n\t"
  "v_exp_f32_e32 v148, v148\n\t"
  "v_mfma_f32_16x16x32_bf16 v[240:243], v[232:235], a[144:147], v[240:243]\n\t"
  "buffer_load_dwordx4 a[144:147], v200, %7, 0 offen offset:1024\n\t"
  "v_mad_u32_u16 v201, v208, s90, v227 op_sel:[1,0,0,0]\n\t"
  "v_mfma_f32_16x16x32_bf16 v[64:67], a[96:99], v[184:187], v[64:67]\n\t"
  "v_mfma_f32_16x16x32_bf16 v[68:71], a[100:103], v[184:187], v[68:71]\n\t"
  "v_exp_f32_e64 v149, v149\n\t"
  "v_mfma_f32_16x16x32_bf16 v[72:75], a[104:107], v[184:187], v[72:75]\n\t"
  "ds_read_b128 a[80:83], v217 offset:8192\n\t"
  "v_cvt_pk_bf16_f32 v170, v148, v149\n\t"
  "v_mfma_f32_16x16x32_bf16 v[76:79], a[108:111], v[184:187], v[76:79]\n\t"
  "v_exp_f32_e64 v150, v150\n\t"
  "v_mfma_f32_16x16x32_bf16 v[80:83], a[112:115], v[184:187], v[80:83]\n\t"
  "buffer_load_dwordx4 v223, %7, s61 offen offset:3072 lds\n\t"
  "v_mfma_f32_16x16x32_bf16 v[84:87], a[116:119], v[184:187], v[84:87]\n\t"
  "s_add_u32 s63, s56, 0x2000\n\t"
  "v_exp_f32_e64 v151, v151\n\t"
  "v_mfma_f32_16x16x32_bf16 v[88:91], a[120:123], v[184:187], v[88:91]\n\t"
  "s_add_u32 m0, s57, 0x2000\n\t"
  "ds_read_b128 a[84:87], v217 offset:8704\n\t"
  "v_cvt_pk_bf16_f32 v171, v150, v151\n\t"
  "v_mfma_f32_16x16x32_bf16 v[92:95], a[124:127], v[184:187], v[92:95]\n\t"
  "v_exp_f32_e64 v152, v152\n\t"
  "v_mfma_f32_16x16x32_bf16 v[240:243], v[232:235], a[148:151], v[240:243]\n\t"
  "buffer_load_dwordx4 a[148:151], v201, %7, 0 offen offset:1024\n\t"
  "v_mad_u32_u16 v200, v209, s90, v227\n\t"
  "v_mfma_f32_16x16x32_bf16 v[96:99], a[96:99], v[188:191], v[96:99]\n\t"
  "ds_read_b128 a[92:95], v217 offset:9728\n\t"
  "v_mfma_f32_16x16x32_bf16 v[100:103], a[100:103], v[188:191], v[100:103]\n\t"
  "v_exp_f32_e64 v153, v153\n\t"
  "v_mfma_f32_16x16x32_bf16 v[104:107], a[104:107], v[188:191], v[104:107]\n\t"
  "ds_read_b128 a[88:91], v217 offset:9216\n\t"
  "v_cvt_pk_bf16_f32 v172, v152, v153\n\t"
  "v_mfma_f32_16x16x32_bf16 v[108:111], a[108:111], v[188:191], v[108:111]\n\t"
  "v_exp_f32_e64 v154, v154\n\t"
  "v_mfma_f32_16x16x32_bf16 v[112:115], a[112:115], v[188:191], v[112:115]\n\t"
  "buffer_load_dwordx4 v224, %8, s63 offen offset:0 lds\n\t"
  "v_mfma_f32_16x16x32_bf16 v[116:119], a[116:119], v[188:191], v[116:119]\n\t"
  "s_waitcnt vmcnt(49)\n\t"
  "v_exp_f32_e32 v155, v155\n\t"
  "v_mfma_f32_16x16x32_bf16 v[120:123], a[120:123], v[188:191], v[120:123]\n\t"
  "v_cvt_pk_bf16_f32 v173, v154, v155\n\t"
  "v_mfma_f32_16x16x32_bf16 v[124:127], a[124:127], v[188:191], v[124:127]\n\t"
  "s_waitcnt lgkmcnt(2)\n\t"
  "v_exp_f32_e32 v156, v156\n\t"
  "v_mfma_f32_16x16x32_bf16 v[240:243], v[232:235], a[152:155], v[240:243]\n\t"
  "buffer_load_dwordx4 a[152:155], v200, %7, 0 offen offset:1024\n\t"
  "v_mad_u32_u16 v201, v209, s90, v227 op_sel:[1,0,0,0]\n\t"
  "v_mfma_f32_16x16x32_bf16 v[128:131], a[64:67], a[0:3], 0\n\t"
  "ds_read_b64_tr_b16 a[96:97], v218 offset:0\n\t"
  "ds_read_b64_tr_b16 a[98:99], v219 offset:0\n\t"
  "v_mfma_f32_16x16x32_bf16 v[128:131], a[68:71], a[4:7], v[128:131]\n\t"
  "v_exp_f32_e64 v157, v157\n\t"
  "v_mfma_f32_16x16x32_bf16 v[128:131], a[72:75], a[8:11], v[128:131]\n\t"
  "ds_read_b64_tr_b16 a[100:101], v196 offset:0\n\t"
  "v_cvt_pk_bf16_f32 v174, v156, v157\n\t"
  "v_mfma_f32_16x16x32_bf16 v[128:131], a[76:79], a[12:15], v[128:131]\n\t"
  "v_exp_f32_e64 v158, v158\n\t"
  "v_mfma_f32_16x16x32_bf16 v[132:135], a[80:83], a[0:3], 0\n\t"
  "buffer_load_dwordx4 v224, %8, s63 offen offset:1024 lds\n\t"
  "v_mfma_f32_16x16x32_bf16 v[132:135], a[84:87], a[4:7], v[132:135]\n\t"
  "s_waitcnt lgkmcnt(3)\n\t"
  "v_exp_f32_e32 v159, v159\n\t"
  "v_mfma_f32_16x16x32_bf16 v[132:135], a[88:91], a[8:11], v[132:135]\n\t"
  "ds_read_b64_tr_b16 a[102:103], v197 offset:0\n\t"
  "v_cvt_pk_bf16_f32 v175, v158, v159\n\t"
  "v_mfma_f32_16x16x32_bf16 v[132:135], a[92:95], a[12:15], v[132:135]\n\t"
  "v_exp_f32_e64 v128, v128\n\t"
  "v_mfma_f32_16x16x32_bf16 v[240:243], v[232:235], a[156:159], v[240:243]\n\t"
  "buffer_load_dwordx4 a[156:159], v201, %7, 0 offen offset:1024\n\t"
  "v_mad_u32_u16 v200, v210, s90, v227\n\t"
  "v_mfma_f32_16x16x32_bf16 v[136:139], a[64:67], a[16:19], 0\n\t"
  "ds_read_b64_tr_b16 a[104:105], v218 offset:512\n\t"
  "ds_read_b64_tr_b16 a[106:107], v219 offset:512\n\t"
  "v_mfma_f32_16x16x32_bf16 v[136:139], a[68:71], a[20:23], v[136:139]\n\t"
  "v_exp_f32_e64 v129, v129\n\t"
  "v_mfma_f32_16x16x32_bf16 v[136:139], a[72:75], a[24:27], v[136:139]\n\t"
  "ds_read_b64_tr_b16 a[108:109], v196 offset:512\n\t"
  "v_cvt_pk_bf16_f32 v176, v128, v129\n\t"
  "v_mfma_f32_16x16x32_bf16 v[136:139], a[76:79], a[28:31], v[136:139]\n\t"
  "v_exp_f32_e64 v130, v130\n\t"
  "v_mfma_f32_16x16x32_bf16 v[140:143], a[80:83], a[16:19], 0\n\t"
  "buffer_load_dwordx4 v224, %8, s63 offen offset:2048 lds\n\t"
  "v_mfma_f32_16x16x32_bf16 v[140:143], a[84:87], a[20:23], v[140:143]\n\t"
  "v_exp_f32_e64 v131, v131\n\t"
  "v_mfma_f32_16x16x32_bf16 v[140:143], a[88:91], a[24:27], v[140:143]\n\t"
  "ds_read_b64_tr_b16 a[110:111], v197 offset:512\n\t"
  "v_cvt_pk_bf16_f32 v177, v130, v131\n\t"
  "v_mfma_f32_16x16x32_bf16 v[140:143], a[92:95], a[28:31], v[140:143]\n\t"
  "s_waitcnt vmcnt(51)\n\t"
  "v_exp_f32_e32 v132, v132\n\t"
  "v_mfma_f32_16x16x32_bf16 v[240:243], v[236:239], a[160:163], v[240:243]\n\t"
  "buffer_load_dwordx4 a[160:163], v200, %7, 0 offen offset:1024\n\t"
  "v_mad_u32_u16 v201, v210, s90, v227 op_sel:[1,0,0,0]\n\t"
  "v_mfma_f32_16x16x32_bf16 v[144:147], a[64:67], a[32:35], 0\n\t"
  "ds_read_b64_tr_b16 a[112:113], v218 offset:1024\n\t"
  "ds_read_b64_tr_b16 a[114:115], v219 offset:1024\n\t"
  "v_mfma_f32_16x16x32_bf16 v[144:147], a[68:71], a[36:39], v[144:147]\n\t"
  "v_exp_f32_e64 v133, v133\n\t"
  "v_mfma_f32_16x16x32_bf16 v[144:147], a[72:75], a[40:43], v[144:147]\n\t"
  "ds_read_b64_tr_b16 a[116:117], v196 offset:1024\n\t"
  "v_cvt_pk_bf16_f32 v178, v132, v133\n\t"
  "v_mfma_f32_16x16x32_bf16 v[144:147], a[76:79], a[44:47], v[144:147]\n\t"
  "v_exp_f32_e64 v134, v134\n\t"
  "v_mfma_f32_16x16x32_bf16 v[148:151], a[80:83], a[32:35], 0\n\t"
  "buffer_load_dwordx4 v224, %8, s63 offen offset:3072 lds\n\t"
  "v_mfma_f32_16x16x32_bf16 v[148:151], a[84:87], a[36:39], v[148:151]\n\t"
  "v_exp_f32_e64 v135, v135\n\t"
  "v_mfma_f32_16x16x32_bf16 v[148:151], a[88:91], a[40:43], v[148:151]\n\t"
  "ds_read_b64_tr_b16 a[118:119], v197 offset:1024\n\t"
  "v_cvt_pk_bf16_f32 v179, v134, v135\n\t"
  "v_mfma_f32_16x16x32_bf16 v[148:151], a[92:95], a[44:47], v[148:151]\n\t"
  "v_exp_f32_e64 v136, v136\n\t"
  "v_mfma_f32_16x16x32_bf16 v[240:243], v[236:239], a[164:167], v[240:243]\n\t"
  "buffer_load_dwordx4 a[164:167], v201, %7, 0 offen offset:1024\n\t"
  "v_mad_u32_u16 v200, v211, s90, v227\n\t"
  "v_mfma_f32_16x16x32_bf16 v[152:155], a[64:67], a[48:51], 0\n\t"
  "ds_read_b64_tr_b16 a[120:121], v218 offset:1536\n\t"
  "ds_read_b64_tr_b16 a[122:123], v219 offset:1536\n\t"
  "v_mfma_f32_16x16x32_bf16 v[152:155], a[68:71], a[52:55], v[152:155]\n\t"
  "v_exp_f32_e64 v137, v137\n\t"
  "v_mfma_f32_16x16x32_bf16 v[152:155], a[72:75], a[56:59], v[152:155]\n\t"
  "ds_read_b64_tr_b16 a[124:125], v196 offset:1536\n\t"
  "v_cvt_pk_bf16_f32 v180, v136, v137\n\t"
  "v_mfma_f32_16x16x32_bf16 v[152:155], a[76:79], a[60:63], v[152:155]\n\t"
  "v_exp_f32_e64 v138, v138\n\t"
  "v_mfma_f32_16x16x32_bf16 v[156:159], a[80:83], a[48:51], 0\n\t"
  "s_waitcnt vmcnt(29)\n\t"
  "s_barrier\n\t"
  "v_mfma_f32_16x16x32_bf16 v[156:159], a[84:87], a[52:55], v[156:159]\n\t"
  "s_waitcnt vmcnt(51)\n\t"
  "v_exp_f32_e32 v139, v139\n\t"
  "v_mfma_f32_16x16x32_bf16 v[156:159], a[88:91], a[56:59], v[156:159]\n\t"
  "ds_read_b64_tr_b16 a[126:127], v197 offset:1536\n\t"
  "v_cvt_pk_bf16_f32 v181, v138, v139\n\t"
  "v_mfma_f32_16x16x32_bf16 v[156:159], a[92:95], a[60:63], v[156:159]\n\t"
  "s_waitcnt lgkmcnt(2)\n\t"
  "v_exp_f32_e32 v140, v140\n\t"
  "v_mfma_f32_16x16x32_bf16 v[240:243], v[236:239], a[168:171], v[240:243]\n\t"
  "buffer_load_dwordx4 a[168:171], v200, %7, 0 offen offset:1024\n\t"
  "v_mad_u32_u16 v201, v211, s90, v227 op_sel:[1,0,0,0]\n\t"
  "v_mfma_f32_16x16x32_bf16 v[0:3], a[96:99], v[160:163], v[0:3]\n\t"
  "s_add_u32 m0, s54, 0xc000\n\t"
  "ds_read_b128 v[206:209], v229 offset:896\n\t"
  "v_mfma_f32_16x16x32_bf16 v[4:7], a[100:103], v[160:163], v[4:7]\n\t"
  "s_mov_b32 s61, 0x30c000\n\t"
  "v_exp_f32_e64 v141, v141\n\t"
  "v_mfma_f32_16x16x32_bf16 v[8:11], a[104:107], v[160:163], v[8:11]\n\t"
  "ds_read_b128 a[64:67], v216 offset:16384\n\t"
  "v_cvt_pk_bf16_f32 v182, v140, v141\n\t"
  "v_mfma_f32_16x16x32_bf16 v[12:15], a[108:111], v[160:163], v[12:15]\n\t"
  "v_exp_f32_e64 v142, v142\n\t"
  "v_mfma_f32_16x16x32_bf16 v[16:19], a[112:115], v[160:163], v[16:19]\n\t"
  "v_mfma_f32_16x16x32_bf16 v[20:23], a[116:119], v[160:163], v[20:23]\n\t"
  "s_waitcnt lgkmcnt(2)\n\t"
  "v_exp_f32_e32 v143, v143\n\t"
  "v_mfma_f32_16x16x32_bf16 v[24:27], a[120:123], v[160:163], v[24:27]\n\t"
  "ds_read_b128 a[68:71], v216 offset:16896\n\t"
  "v_cvt_pk_bf16_f32 v183, v142, v143\n\t"
  "v_mfma_f32_16x16x32_bf16 v[28:31], a[124:127], v[160:163], v[28:31]\n\t"
  "s_waitcnt lgkmcnt(2)\n\t"
  "v_exp_f32_e32 v144, v144\n\t"
  "v_mfma_f32_16x16x32_bf16 v[240:243], v[236:239], a[172:175], v[240:243]\n\t"
  "buffer_load_dwordx4 a[172:175], v201, %7, 0 offen offset:1024\n\t"
  "v_mad_u32_u16 v200, v212, s90, v227\n\t"
  "v_mfma_f32_16x16x32_bf16 v[32:35], a[96:99], v[164:167], v[32:35]\n\t"
  "v_mfma_f32_16x16x32_bf16 v[36:39], a[100:103], v[164:167], v[36:39]\n\t"
  "v_exp_f32_e64 v145, v145\n\t"
  "v_mfma_f32_16x16x32_bf16 v[40:43], a[104:107], v[164:167], v[40:43]\n\t"
  "ds_read_b128 a[72:75], v216 offset:17408\n\t"
  "v_cvt_pk_bf16_f32 v184, v144, v145\n\t"
  "v_mfma_f32_16x16x32_bf16 v[44:47], a[108:111], v[164:167], v[44:47]\n\t"
  "v_exp_f32_e64 v146, v146\n\t"
  "v_mfma_f32_16x16x32_bf16 v[48:51], a[112:115], v[164:167], v[48:51]\n\t"
  "buffer_load_dwordx4 v220, %7, s61 offen offset:0 lds\n\t"
  "v_mfma_f32_16x16x32_bf16 v[52:55], a[116:119], v[164:167], v[52:55]\n\t"
  "v_exp_f32_e64 v147, v147\n\t"
  "v_mfma_f32_16x16x32_bf16 v[56:59], a[120:123], v[164:167], v[56:59]\n\t"
  "ds_read_b128 a[76:79], v216 offset:17920\n\t"
  "v_cvt_pk_bf16_f32 v185, v146, v147\n\t"
  "v_mfma_f32_16x16x32_bf16 v[60:63], a[124:127], v[164:167], v[60:63]\n\t"
  "s_waitcnt vmcnt(47)\n\t"
  "v_exp_f32_e32 v148, v148\n\t"
  "v_mfma_f32_16x16x32_bf16 v[240:243], v[236:239], a[176:179], v[240:243]\n\t"
  "buffer_load_dwordx4 a[176:179], v200, %7, 0 offen offset:1024\n\t"
  "v_mad_u32_u16 v201, v212, s90, v227 op_sel:[1,0,0,0]\n\t"
  "v_mfma_f32_16x16x32_bf16 v[64:67], a[96:99], v[168:171], v[64:67]\n\t"
  "v_mfma_f32_16x16x32_bf16 v[68:71], a[100:103], v[168:171], v[68:71]\n\t"
  "v_exp_f32_e64 v149, v149\n\t"
  "v_mfma_f32_16x16x32_bf16 v[72:75], a[104:107], v[168:171], v[72:75]\n\t"
  "ds_read_b128 a[80:83], v217 offset:16384\n\t"
  "v_cvt_pk_bf16_f32 v186, v148, v149\n\t"
  "v_mfma_f32_16x16x32_bf16 v[76:79], a[108:111], v[168:171], v[76:79]\n\t"
  "v_exp_f32_e64 v150, v150\n\t"
  "v_mfma_f32_16x16x32_bf16 v[80:83], a[112:115], v[168:171], v[80:83]\n\t"
  "buffer_load_dwordx4 v221, %7, s61 offen offset:1024 lds\n\t"
  "v_mfma_f32_16x16x32_bf16 v[84:87], a[116:119], v[168:171], v[84:87]\n\t"
  "v_exp_f32_e64 v151, v151\n\t"
  "v_mfma_f32_16x16x32_bf16 v[88:91], a[120:123], v[168:171], v[88:91]\n\t"
  "ds_read_b128 a[84:87], v217 offset:16896\n\t"
  "v_cvt_pk_bf16_f32 v187, v150, v151\n\t"
  "v_mfma_f32_16x16x32_bf16 v[92:95], a[124:127], v[168:171], v[92:95]\n\t"
  "v_exp_f32_e64 v152, v152\n\t"
  "v_mfma_f32_16x16x32_bf16 v[240:243], v[236:239], a[180:183], v[240:243]\n\t"
  "buffer_load_dwordx4 a[180:183], v201, %7, 0 offen offset:1024\n\t"
  "v_mad_u32_u16 v200, v213, s90, v227\n\t"
  "v_mfma_f32_16x16x32_bf16 v[96:99], a[96:99], v[172:175], v[96:99]\n\t"
  "ds_read_b128 a[92:95], v217 offset:17920\n\t"
  "v_mfma_f32_16x16x32_bf16 v[100:103], a[100:103], v[172:175], v[100:103]\n\t"
  "v_exp_f32_e64 v153, v153\n\t"
  "v_mfma_f32_16x16x32_bf16 v[104:107], a[104:107], v[172:175], v[104:107]\n\t"
  "ds_read_b128 a[88:91], v217 offset:17408\n\t"
  "v_cvt_pk_bf16_f32 v188, v152, v153\n\t"
  "v_mfma_f32_16x16x32_bf16 v[108:111], a[108:111], v[172:175], v[108:111]\n\t"
  "v_exp_f32_e64 v154, v154\n\t"
  "v_mfma_f32_16x16x32_bf16 v[112:115], a[112:115], v[172:175], v[112:115]\n\t"
  "v_mfma_f32_16x16x32_bf16 v[116:119], a[116:119], v[172:175], v[116:119]\n\t"
  "s_waitcnt vmcnt(47)\n\t"
  "v_exp_f32_e32 v155, v155\n\t"
  "v_mfma_f32_16x16x32_bf16 v[120:123], a[120:123], v[172:175], v[120:123]\n\t"
  "v_cvt_pk_bf16_f32 v189, v154, v155\n\t"
  "v_mfma_f32_16x16x32_bf16 v[124:127], a[124:127], v[172:175], v[124:127]\n\t"
  "s_waitcnt lgkmcnt(2)\n\t"
  "v_exp_f32_e32 v156, v156\n\t"
  "v_mfma_f32_16x16x32_bf16 v[240:243], v[236:239], a[184:187], v[240:243]\n\t"
  "buffer_load_dwordx4 a[184:187], v200, %7, 0 offen offset:1024\n\t"
  "v_mad_u32_u16 v201, v213, s90, v227 op_sel:[1,0,0,0]\n\t"
  "v_mfma_f32_16x16x32_bf16 v[128:131], a[64:67], a[0:3], 0\n\t"
  "ds_read_b64_tr_b16 a[96:97], v218 offset:8192\n\t"
  "ds_read_b64_tr_b16 a[98:99], v219 offset:8192\n\t"
  "v_mfma_f32_16x16x32_bf16 v[128:131], a[68:71], a[4:7], v[128:131]\n\t"
  "v_exp_f32_e64 v157, v157\n\t"
  "v_mfma_f32_16x16x32_bf16 v[128:131], a[72:75], a[8:11], v[128:131]\n\t"
  "ds_read_b64_tr_b16 a[100:101], v196 offset:8192\n\t"
  "v_cvt_pk_bf16_f32 v190, v156, v157\n\t"
  "v_mfma_f32_16x16x32_bf16 v[128:131], a[76:79], a[12:15], v[128:131]\n\t"
  "v_exp_f32_e64 v158, v158\n\t"
  "v_mfma_f32_16x16x32_bf16 v[132:135], a[80:83], a[0:3], 0\n\t"
  "ds_read_b128 v[210:213], v229 offset:1024\n\t"
  "v_mfma_f32_16x16x32_bf16 v[132:135], a[84:87], a[4:7], v[132:135]\n\t"
  "s_waitcnt lgkmcnt(4)\n\t"
  "v_exp_f32_e32 v159, v159\n\t"
  "v_mfma_f32_16x16x32_bf16 v[132:135], a[88:91], a[8:11], v[132:135]\n\t"
  "ds_read_b64_tr_b16 a[102:103], v197 offset:8192\n\t"
  "v_cvt_pk_bf16_f32 v191, v158, v159\n\t"
  "v_mfma_f32_16x16x32_bf16 v[132:135], a[92:95], a[12:15], v[132:135]\n\t"
  "v_exp_f32_e64 v128, v128\n\t"
  "v_mfma_f32_16x16x32_bf16 v[240:243], v[236:239], a[188:191], v[240:243]\n\t"
  "buffer_load_dwordx4 a[188:191], v201, %7, 0 offen offset:1024\n\t"
  "v_mad_u32_u16 v200, v202, s90, v227\n\t"
  "v_mfma_f32_16x16x32_bf16 v[136:139], a[64:67], a[16:19], 0\n\t"
  "ds_read_b64_tr_b16 a[104:105], v218 offset:8704\n\t"
  "ds_read_b64_tr_b16 a[106:107], v219 offset:8704\n\t"
  "v_mfma_f32_16x16x32_bf16 v[136:139], a[68:71], a[20:23], v[136:139]\n\t"
  "s_waitcnt lgkmcnt(3)\n\t"
  "v_exp_f32_e32 v129, v129\n\t"
  "v_mfma_f32_16x16x32_bf16 v[136:139], a[72:75], a[24:27], v[136:139]\n\t"
  "ds_read_b64_tr_b16 a[108:109], v196 offset:8704\n\t"
  "v_cvt_pk_bf16_f32 v160, v128, v129\n\t"
  "v_mfma_f32_16x16x32_bf16 v[136:139], a[76:79], a[28:31], v[136:139]\n\t"
  "v_exp_f32_e64 v130, v130\n\t"
  "v_mfma_f32_16x16x32_bf16 v[140:143], a[80:83], a[16:19], 0\n\t"
  "v_mfma_f32_16x16x32_bf16 v[140:143], a[84:87], a[20:23], v[140:143]\n\t"
  "v_exp_f32_e64 v131, v131\n\t"
  "v_mfma_f32_16x16x32_bf16 v[140:143], a[88:91], a[24:27], v[140:143]\n\t"
  "ds_read_b64_tr_b16 a[110:111], v197 offset:8704\n\t"
  "v_cvt_pk_bf16_f32 v161, v130, v131\n\t"
  "v_mfma_f32_16x16x32_bf16 v[140:143], a[92:95], a[28:31], v[140:143]\n\t"
  "s_waitcnt vmcnt(46)\n\t"
  "v_exp_f32_e32 v132, v132\n\t"
  "v_mfma_f32_16x16x32_bf16 v[244:247], v[232:235], a[192:195], 0\n\t"
  "buffer_load_dwordx4 a[192:195], v200, %7, 0 offen offset:1024\n\t"
  "v_mad_u32_u16 v201, v202, s90, v227 op_sel:[1,0,0,0]\n\t"
  "v_mfma_f32_16x16x32_bf16 v[144:147], a[64:67], a[32:35], 0\n\t"
  "ds_read_b64_tr_b16 a[112:113], v218 offset:9216\n\t"
  "ds_read_b64_tr_b16 a[114:115], v219 offset:9216\n\t"
  "v_mfma_f32_16x16x32_bf16 v[144:147], a[68:71], a[36:39], v[144:147]\n\t"
  "v_exp_f32_e64 v133, v133\n\t"
  "v_mfma_f32_16x16x32_bf16 v[144:147], a[72:75], a[40:43], v[144:147]\n\t"
  "ds_read_b64_tr_b16 a[116:117], v196 offset:9216\n\t"
  "v_cvt_pk_bf16_f32 v162, v132, v133\n\t"
  "v_mfma_f32_16x16x32_bf16 v[144:147], a[76:79], a[44:47], v[144:147]\n\t"
  "v_exp_f32_e64 v134, v134\n\t"
  "v_mfma_f32_16x16x32_bf16 v[148:151], a[80:83], a[32:35], 0\n\t"
  "v_mfma_f32_16x16x32_bf16 v[148:151], a[84:87], a[36:39], v[148:151]\n\t"
  "v_exp_f32_e64 v135, v135\n\t"
  "v_mfma_f32_16x16x32_bf16 v[148:151], a[88:91], a[40:43], v[148:151]\n\t"
  "ds_read_b64_tr_b16 a[118:119], v197 offset:9216\n\t"
  "v_cvt_pk_bf16_f32 v163, v134, v135\n\t"
  "v_mfma_f32_16x16x32_bf16 v[148:151], a[92:95], a[44:47], v[148:151]\n\t"
  "v_exp_f32_e64 v136, v136\n\t"
  "v_mfma_f32_16x16x32_bf16 v[244:247], v[232:235], a[196:199], v[244:247]\n\t"
  "buffer_load_dwordx4 a[196:199], v201, %7, 0 offen offset:1024\n\t"
  "v_mad_u32_u16 v200, v203, s90, v227\n\t"
  "v_mfma_f32_16x16x32_bf16 v[152:155], a[64:67], a[48:51], 0\n\t"
  "ds_read_b64_tr_b16 a[120:121], v218 offset:9728\n\t"
  "ds_read_b64_tr_b16 a[122:123], v219 offset:9728\n\t"
  "v_mfma_f32_16x16x32_bf16 v[152:155], a[68:71], a[52:55], v[152:155]\n\t"
  "v_exp_f32_e64 v137, v137\n\t"
  "v_mfma_f32_16x16x32_bf16 v[152:155], a[72:75], a[56:59], v[152:155]\n\t"
  "ds_read_b64_tr_b16 a[124:125], v196 offset:9728\n\t"
  "v_cvt_pk_bf16_f32 v164, v136, v137\n\t"
  "v_mfma_f32_16x16x32_bf16 v[152:155], a[76:79], a[60:63], v[152:155]\n\t"
  "v_exp_f32_e64 v138, v138\n\t"
  "v_mfma_f32_16x16x32_bf16 v[156:159], a[80:83], a[48:51], 0\n\t"
  "v_mfma_f32_16x16x32_bf16 v[156:159], a[84:87], a[52:55], v[156:159]\n\t"
  "s_waitcnt vmcnt(46)\n\t"
  "v_exp_f32_e32 v139, v139\n\t"
  "v_mfma_f32_16x16x32_bf16 v[156:159], a[88:91], a[56:59], v[156:159]\n\t"
  "ds_read_b64_tr_b16 a[126:127], v197 offset:9728\n\t"
  "v_cvt_pk_bf16_f32 v165, v138, v139\n\t"
  "v_mfma_f32_16x16x32_bf16 v[156:159], a[92:95], a[60:63], v[156:159]\n\t"
  "s_waitcnt lgkmcnt(2)\n\t"
  "v_exp_f32_e32 v140, v140\n\t"
  "v_mfma_f32_16x16x32_bf16 v[244:247], v[232:235], a[200:203], v[244:247]\n\t"
  "buffer_load_dwordx4 a[200:203], v200, %7, 0 offen offset:1024\n\t"
  "v_mad_u32_u16 v201, v203, s90, v227 op_sel:[1,0,0,0]\n\t"
  "v_mfma_f32_16x16x32_bf16 v[0:3], a[96:99], v[176:179], v[0:3]\n\t"
  "v_mfma_f32_16x16x32_bf16 v[4:7], a[100:103], v[176:179], v[4:7]\n\t"
  "v_exp_f32_e64 v141, v141\n\t"
  "v_mfma_f32_16x16x32_bf16 v[8:11], a[104:107], v[176:179], v[8:11]\n\t"
  "ds_read_b128 a[64:67], v216 offset:24576\n\t"
  "v_cvt_pk_bf16_f32 v166, v140, v141\n\t"
  "v_mfma_f32_16x16x32_bf16 v[12:15], a[108:111], v[176:179], v[12:15]\n\t"
  "v_exp_f32_e64 v142, v142\n\t"
  "v_mfma_f32_16x16x32_bf16 v[16:19], a[112:115], v[176:179], v[16:19]\n\t"
  "v_mfma_f32_16x16x32_bf16 v[20:23], a[116:119], v[176:179], v[20:23]\n\t"
  "s_waitcnt lgkmcnt(1)\n\t"
  "v_exp_f32_e32 v143, v143\n\t"
  "v_mfma_f32_16x16x32_bf16 v[24:27], a[120:123], v[176:179], v[24:27]\n\t"
  "ds_read_b128 a[68:71], v216 offset:25088\n\t"
  "v_cvt_pk_bf16_f32 v167, v142, v143\n\t"
  "v_mfma_f32_16x16x32_bf16 v[28:31], a[124:127], v[176:179], v[28:31]\n\t"
  "v_exp_f32_e64 v144, v144\n\t"
  "v_mfma_f32_16x16x32_bf16 v[244:247], v[232:235], a[204:207], v[244:247]\n\t"
  "buffer_load_dwordx4 a[204:207], v201, %7, 0 offen offset:1024\n\t"
  "v_mad_u32_u16 v200, v204, s90, v227\n\t"
  "v_mfma_f32_16x16x32_bf16 v[32:35], a[96:99], v[180:183], v[32:35]\n\t"
  "v_mfma_f32_16x16x32_bf16 v[36:39], a[100:103], v[180:183], v[36:39]\n\t"
  "v_exp_f32_e64 v145, v145\n\t"
  "v_mfma_f32_16x16x32_bf16 v[40:43], a[104:107], v[180:183], v[40:43]\n\t"
  "ds_read_b128 a[72:75], v216 offset:25600\n\t"
  "v_cvt_pk_bf16_f32 v168, v144, v145\n\t"
  "v_mfma_f32_16x16x32_bf16 v[44:47], a[108:111], v[180:183], v[44:47]\n\t"
  "v_exp_f32_e64 v146, v146\n\t"
  "v_mfma_f32_16x16x32_bf16 v[48:51], a[112:115], v[180:183], v[48:51]\n\t"
  "buffer_load_dwordx4 v222, %7, s61 offen offset:2048 lds\n\t"
  "v_mfma_f32_16x16x32_bf16 v[52:55], a[116:119], v[180:183], v[52:55]\n\t"
  "v_exp_f32_e64 v147, v147\n\t"
  "v_mfma_f32_16x16x32_bf16 v[56:59], a[120:123], v[180:183], v[56:59]\n\t"
  "ds_read_b128 a[76:79], v216 offset:26112\n\t"
  "v_cvt_pk_bf16_f32 v169, v146, v147\n\t"
  "v_mfma_f32_16x16x32_bf16 v[60:63], a[124:127], v[180:183], v[60:63]\n\t"
  "s_waitcnt vmcnt(47)\n\t"
  "v_exp_f32_e32 v148, v148\n\t"
  "v_mfma_f32_16x16x32_bf16 v[244:247], v[232:235], a[208:211], v[244:247]\n\t"
  "buffer_load_dwordx4 a[208:211], v200, %7, 0 offen offset:1024\n\t"
  "v_mad_u32_u16 v201, v204, s90, v227 op_sel:[1,0,0,0]\n\t"
  "v_mfma_f32_16x16x32_bf16 v[64:67], a[96:99], v[184:187], v[64:67]\n\t"
  "v_mfma_f32_16x16x32_bf16 v[68:71], a[100:103], v[184:187], v[68:71]\n\t"
  "v_exp_f32_e64 v149, v149\n\t"
  "v_mfma_f32_16x16x32_bf16 v[72:75], a[104:107], v[184:187], v[72:75]\n\t"
  "ds_read_b128 a[80:83], v217 offset:24576\n\t"
  "v_cvt_pk_bf16_f32 v170, v148, v149\n\t"
  "v_mfma_f32_16x16x32_bf16 v[76:79], a[108:111], v[184:187], v[76:79]\n\t"
  "v_exp_f32_e64 v150, v150\n\t"
  "v_mfma_f32_16x16x32_bf16 v[80:83], a[112:115], v[184:187], v[80:83]\n\t"
  "buffer_load_dwordx4 v223, %7, s61 offen offset:3072 lds\n\t"
  "v_mfma_f32_16x16x32_bf16 v[84:87], a[116:119], v[184:187], v[84:87]\n\t"
  "s_add_u32 s63, s56, 0x3000\n\t"
  "v_exp_f32_e64 v151, v151\n\t"
  "v_mfma_f32_16x16x32_bf16 v[88:91], a[120:123], v[184:187], v[88:91]\n\t"
  "s_add_u32 m0, s57, 0x3000\n\t"
  "ds_read_b128 a[84:87], v217 offset:25088\n\t"
  "v_cvt_pk_bf16_f32 v171, v150, v151\n\t"
  "v_mfma_f32_16x16x32_bf16 v[92:95], a[124:127], v[184:187], v[92:95]\n\t"
  "v_exp_f32_e64 v152, v152\n\t"
  "v_mfma_f32_16x16x32_bf16 v[244:247], v[232:235], a[212:215], v[244:247]\n\t"
  "buffer_load_dwordx4 a[212:215], v201, %7, 0 offen offset:1024\n\t"
  "v_mad_u32_u16 v200, v205, s90, v227\n\t"
  "v_mfma_f32_16x16x32_bf16 v[96:99], a[96:99], v[188:191], v[96:99]\n\t"
  "ds_read_b128 a[92:95], v217 offset:26112\n\t"
  "v_mfma_f32_16x16x32_bf16 v[100:103], a[100:103], v[188:191], v[100:103]\n\t"
  "v_exp_f32_e64 v153, v153\n\t"
  "v_mfma_f32_16x16x32_bf16 v[104:107], a[104:107], v[188:191], v[104:107]\n\t"
  "ds_read_b128 a[88:91], v217 offset:25600\n\t"
  "v_cvt_pk_bf16_f32 v172, v152, v153\n\t"
  "v_mfma_f32_16x16x32_bf16 v[108:111], a[108:111], v[188:191], v[108:111]\n\t"
  "v_exp_f32_e64 v154, v154\n\t"
  "v_mfma_f32_16x16x32_bf16 v[112:115], a[112:115], v[188:191], v[112:115]\n\t"
  "buffer_load_dwordx4 v224, %8, s63 offen offset:0 lds\n\t"
  "v_mfma_f32_16x16x32_bf16 v[116:119], a[116:119], v[188:191], v[116:119]\n\t"
  "s_waitcnt vmcnt(49)\n\t"
  "v_exp_f32_e32 v155, v155\n\t"
  "v_mfma_f32_16x16x32_bf16 v[120:123], a[120:123], v[188:191], v[120:123]\n\t"
  "v_cvt_pk_bf16_f32 v173, v154, v155\n\t"
  "v_mfma_f32_16x16x32_bf16 v[124:127], a[124:127], v[188:191], v[124:127]\n\t"
  "s_waitcnt lgkmcnt(2)\n\t"
  "v_exp_f32_e32 v156, v156\n\t"
  "v_mfma_f32_16x16x32_bf16 v[244:247], v[232:235], a[216:219], v[244:247]\n\t"
  "buffer_load_dwordx4 a[216:219], v200, %7, 0 offen offset:1024\n\t"
  "v_mad_u32_u16 v201, v205, s90, v227 op_sel:[1,0,0,0]\n\t"
  "v_mfma_f32_16x16x32_bf16 v[128:131], a[64:67], a[0:3], 0\n\t"
  "ds_read_b64_tr_b16 a[96:97], v218 offset:16384\n\t"
  "ds_read_b64_tr_b16 a[98:99], v219 offset:16384\n\t"
  "v_mfma_f32_16x16x32_bf16 v[128:131], a[68:71], a[4:7], v[128:131]\n\t"
  "v_exp_f32_e64 v157, v157\n\t"
  "v_mfma_f32_16x16x32_bf16 v[128:131], a[72:75], a[8:11], v[128:131]\n\t"
  "ds_read_b64_tr_b16 a[100:101], v196 offset:16384\n\t"
  "v_cvt_pk_bf16_f32 v174, v156, v157\n\t"
  "v_mfma_f32_16x16x32_bf16 v[128:131], a[76:79], a[12:15], v[128:131]\n\t"
  "v_exp_f32_e64 v158, v158\n\t"
  "v_mfma_f32_16x16x32_bf16 v[132:135], a[80:83], a[0:3], 0\n\t"
  "buffer_load_dwordx4 v224, %8, s63 offen offset:1024 lds\n\t"
  "v_mfma_f32_16x16x32_bf16 v[132:135], a[84:87], a[4:7], v[132:135]\n\t"
  "s_waitcnt lgkmcnt(3)\n\t"
  "v_exp_f32_e32 v159, v159\n\t"
  "v_mfma_f32_16x16x32_bf16 v[132:135], a[88:91], a[8:11], v[132:135]\n\t"
  "ds_read_b64_tr_b16 a[102:103], v197 offset:16384\n\t"
  "v_cvt_pk_bf16_f32 v175, v158, v159\n\t"
  "v_mfma_f32_16x16x32_bf16 v[132:135], a[92:95], a[12:15], v[132:135]\n\t"
  "v_exp_f32_e64 v128, v128\n\t"
  "v_mfma_f32_16x16x32_bf16 v[244:247], v[232:235], a[220:223], v[244:247]\n\t"
  "buffer_load_dwordx4 a[220:223], v201, %7, 0 offen offset:1024\n\t"
  "v_mad_u32_u16 v200, v206, s90, v227\n\t"
  "v_mfma_f32_16x16x32_bf16 v[136:139], a[64:67], a[16:19], 0\n\t"
  "ds_read_b64_tr_b16 a[104:105], v218 offset:16896\n\t"
  "ds_read_b64_tr_b16 a[106:107], v219 offset:16896\n\t"
  "v_mfma_f32_16x16x32_bf16 v[136:139], a[68:71], a[20:23], v[136:139]\n\t"
  "v_exp_f32_e64 v129, v129\n\t"
  "v_mfma_f32_16x16x32_bf16 v[136:139], a[72:75], a[24:27], v[136:139]\n\t"
  "ds_read_b64_tr_b16 a[108:109], v196 offset:16896\n\t"
  "v_cvt_pk_bf16_f32 v176, v128, v129\n\t"
  "v_mfma_f32_16x16x32_bf16 v[136:139], a[76:79], a[28:31], v[136:139]\n\t"
  "v_exp_f32_e64 v130, v130\n\t"
  "v_mfma_f32_16x16x32_bf16 v[140:143], a[80:83], a[16:19], 0\n\t"
  "buffer_load_dwordx4 v224, %8, s63 offen offset:2048 lds\n\t"
  "v_mfma_f32_16x16x32_bf16 v[140:143], a[84:87], a[20:23], v[140:143]\n\t"
  "v_exp_f32_e64 v131, v131\n\t"
  "v_mfma_f32_16x16x32_bf16 v[140:143], a[88:91], a[24:27], v[140:143]\n\t"
  "ds_read_b64_tr_b16 a[110:111], v197 offset:16896\n\t"
  "v_cvt_pk_bf16_f32 v177, v130, v131\n\t"
  "v_mfma_f32_16x16x32_bf16 v[140:143], a[92:95], a[28:31], v[140:143]\n\t"
  "s_waitcnt vmcnt(45)\n\t"
  "v_exp_f32_e32 v132, v132\n\t"
  "v_mfma_f32_16x16x32_bf16 v[244:247], v[236:239], a[224:227], v[244:247]\n\t"
  "buffer_load_dwordx4 a[224:227], v200, %7, 0 offen offset:1024\n\t"
  "v_mad_u32_u16 v201, v206, s90, v227 op_sel:[1,0,0,0]\n\t"
  "v_mfma_f32_16x16x32_bf16 v[144:147], a[64:67], a[32:35], 0\n\t"
  "ds_read_b64_tr_b16 a[112:113], v218 offset:17408\n\t"
  "ds_read_b64_tr_b16 a[114:115], v219 offset:17408\n\t"
  "v_mfma_f32_16x16x32_bf16 v[144:147], a[68:71], a[36:39], v[144:147]\n\t"
  "v_exp_f32_e64 v133, v133\n\t"
  "v_mfma_f32_16x16x32_bf16 v[144:147], a[72:75], a[40:43], v[144:147]\n\t"
  "ds_read_b64_tr_b16 a[116:117], v196 offset:17408\n\t"
  "v_cvt_pk_bf16_f32 v178, v132, v133\n\t"
  "v_mfma_f32_16x16x32_bf16 v[144:147], a[76:79], a[44:47], v[144:147]\n\t"
  "v_exp_f32_e64 v134, v134\n\t"
  "v_mfma_f32_16x16x32_bf16 v[148:151], a[80:83], a[32:35], 0\n\t"
  "buffer_load_dwordx4 v224, %8, s63 offen offset:3072 lds\n\t"
  "v_mfma_f32_16x16x32_bf16 v[148:151], a[84:87], a[36:39], v[148:151]\n\t"
  "v_exp_f32_e64 v135, v135\n\t"
  "v_mfma_f32_16x16x32_bf16 v[148:151], a[88:91], a[40:43], v[148:151]\n\t"
  "ds_read_b64_tr_b16 a[118:119], v197 offset:17408\n\t"
  "v_cvt_pk_bf16_f32 v179, v134, v135\n\t"
  "v_mfma_f32_16x16x32_bf16 v[148:151], a[92:95], a[44:47], v[148:151]\n\t"
  "v_exp_f32_e64 v136, v136\n\t"
  "v_mfma_f32_16x16x32_bf16 v[244:247], v[236:239], a[228:231], v[244:247]\n\t"
  "buffer_load_dwordx4 a[228:231], v201, %7, 0 offen offset:1024\n\t"
  "v_mad_u32_u16 v200, v207, s90, v227\n\t"
  "v_mfma_f32_16x16x32_bf16 v[152:155], a[64:67], a[48:51], 0\n\t"
  "ds_read_b64_tr_b16 a[120:121], v218 offset:17920\n\t"
  "ds_read_b64_tr_b16 a[122:123], v219 offset:17920\n\t"
  "v_mfma_f32_16x16x32_bf16 v[152:155], a[68:71], a[52:55], v[152:155]\n\t"
  "v_exp_f32_e64 v137, v137\n\t"
  "v_mfma_f32_16x16x32_bf16 v[152:155], a[72:75], a[56:59], v[152:155]\n\t"
  "ds_read_b64_tr_b16 a[124:125], v196 offset:17920\n\t"
  "v_cvt_pk_bf16_f32 v180, v136, v137\n\t"
  "v_mfma_f32_16x16x32_bf16 v[152:155], a[76:79], a[60:63], v[152:155]\n\t"
  "v_exp_f32_e64 v138, v138\n\t"
  "v_mfma_f32_16x16x32_bf16 v[156:159], a[80:83], a[48:51], 0\n\t"
  "s_waitcnt vmcnt(33)\n\t"
  "s_barrier\n\t"
  "v_mfma_f32_16x16x32_bf16 v[156:159], a[84:87], a[52:55], v[156:159]\n\t"
  "s_waitcnt vmcnt(46)\n\t"
  "v_exp_f32_e32 v139, v139\n\t"
  "v_mfma_f32_16x16x32_bf16 v[156:159], a[88:91], a[56:59], v[156:159]\n\t"
  "ds_read_b64_tr_b16 a[126:127], v197 offset:17920\n\t"
  "v_cvt_pk_bf16_f32 v181, v138, v139\n\t"
  "v_mfma_f32_16x16x32_bf16 v[156:159], a[92:95], a[60:63], v[156:159]\n\t"
  "s_waitcnt lgkmcnt(2)\n\t"
  "v_exp_f32_e32 v140, v140\n\t"
  "v_mfma_f32_16x16x32_bf16 v[244:247], v[236:239], a[232:235], v[244:247]\n\t"
  "buffer_load_dwordx4 a[232:235], v200, %7, 0 offen offset:1024\n\t"
  "v_mad_u32_u16 v201, v207, s90, v227 op_sel:[1,0,0,0]\n\t"
  "v_mfma_f32_16x16x32_bf16 v[0:3], a[96:99], v[160:163], v[0:3]\n\t"
  "v_cndmask_b32_e64 v220, v224, -1.0, s[64:65]\n\t"
  "v_mfma_f32_16x16x32_bf16 v[4:7], a[100:103], v[160:163], v[4:7]\n\t"
  "v_exp_f32_e64 v141, v141\n\t"
  "v_mfma_f32_16x16x32_bf16 v[8:11], a[104:107], v[160:163], v[8:11]\n\t"
  "ds_read_b128 a[64:67], v216 offset:32768\n\t"
  "v_cvt_pk_bf16_f32 v182, v140, v141\n\t"
  "v_mfma_f32_16x16x32_bf16 v[12:15], a[108:111], v[160:163], v[12:15]\n\t"
  "v_exp_f32_e64 v142, v142\n\t"
  "v_mfma_f32_16x16x32_bf16 v[16:19], a[112:115], v[160:163], v[16:19]\n\t"
  "v_cndmask_b32_e64 v221, v224, -1.0, s[66:67]\n\t"
  "v_mfma_f32_16x16x32_bf16 v[20:23], a[116:119], v[160:163], v[20:23]\n\t"
  "s_waitcnt lgkmcnt(1)\n\t"
  "v_exp_f32_e32 v143, v143\n\t"
  "v_mfma_f32_16x16x32_bf16 v[24:27], a[120:123], v[160:163], v[24:27]\n\t"
  "ds_read_b128 a[68:71], v216 offset:33280\n\t"
  "v_cvt_pk_bf16_f32 v183, v142, v143\n\t"
  "v_mfma_f32_16x16x32_bf16 v[28:31], a[124:127], v[160:163], v[28:31]\n\t"
  "v_exp_f32_e64 v144, v144\n\t"
  "v_mfma_f32_16x16x32_bf16 v[244:247], v[236:239], a[236:239], v[244:247]\n\t"
  "buffer_load_dwordx4 a[236:239], v201, %7, 0 offen offset:1024\n\t"
  "v_mad_u32_u16 v200, v208, s90, v227\n\t"
  "v_mfma_f32_16x16x32_bf16 v[32:35], a[96:99], v[164:167], v[32:35]\n\t"
  "v_cndmask_b32_e64 v222, v224, -1.0, s[68:69]\n\t"
  "v_mfma_f32_16x16x32_bf16 v[36:39], a[100:103], v[164:167], v[36:39]\n\t"
  "v_exp_f32_e64 v145, v145\n\t"
  "v_mfma_f32_16x16x32_bf16 v[40:43], a[104:107], v[164:167], v[40:43]\n\t"
  "ds_read_b128 a[72:75], v216 offset:33792\n\t"
  "v_cvt_pk_bf16_f32 v184, v144, v145\n\t"
  "v_mfma_f32_16x16x32_bf16 v[44:47], a[108:111], v[164:167], v[44:47]\n\t"
  "v_exp_f32_e64 v146, v146\n\t"
  "v_mfma_f32_16x16x32_bf16 v[48:51], a[112:115], v[164:167], v[48:51]\n\t"
  "v_cndmask_b32_e64 v223, v224, -1.0, s[70:71]\n\t"
  "v_mfma_f32_16x16x32_bf16 v[52:55], a[116:119], v[164:167], v[52:55]\n\t"
  "v_exp_f32_e64 v147, v147\n\t"
  "v_mfma_f32_16x16x32_bf16 v[56:59], a[120:123], v[164:167], v[56:59]\n\t"
  "ds_read_b128 a[76:79], v216 offset:34304\n\t"
  "v_cvt_pk_bf16_f32 v185, v146, v147\n\t"
  "v_mfma_f32_16x16x32_bf16 v[60:63], a[124:127], v[164:167], v[60:63]\n\t"
  "s_waitcnt vmcnt(44)\n\t"
  "v_exp_f32_e32 v148, v148\n\t"
  "v_mfma_f32_16x16x32_bf16 v[244:247], v[236:239], a[240:243], v[244:247]\n\t"
  "buffer_load_dwordx4 a[240:243], v200, %7, 0 offen offset:1024\n\t"
  "v_mad_u32_u16 v201, v208, s90, v227 op_sel:[1,0,0,0]\n\t"
  "v_mfma_f32_16x16x32_bf16 v[64:67], a[96:99], v[168:171], v[64:67]\n\t"
  "v_mfma_f32_16x16x32_bf16 v[68:71], a[100:103], v[168:171], v[68:71]\n\t"
  "v_exp_f32_e64 v149, v149\n\t"
  "v_mfma_f32_16x16x32_bf16 v[72:75], a[104:107], v[168:171], v[72:75]\n\t"
  "ds_read_b128 a[80:83], v217 offset:32768\n\t"
  "v_cvt_pk_bf16_f32 v186, v148, v149\n\t"
  "v_mfma_f32_16x16x32_bf16 v[76:79], a[108:111], v[168:171], v[76:79]\n\t"
  "v_exp_f32_e64 v150, v150\n\t"
  "v_mfma_f32_16x16x32_bf16 v[80:83], a[112:115], v[168:171], v[80:83]\n\t"
  "v_mfma_f32_16x16x32_bf16 v[84:87], a[116:119], v[168:171], v[84:87]\n\t"
  "v_exp_f32_e64 v151, v151\n\t"
  "v_mfma_f32_16x16x32_bf16 v[88:91], a[120:123], v[168:171], v[88:91]\n\t"
  "ds_read_b128 a[84:87], v217 offset:33280\n\t"
  "v_cvt_pk_bf16_f32 v187, v150, v151\n\t"
  "v_mfma_f32_16x16x32_bf16 v[92:95], a[124:127], v[168:171], v[92:95]\n\t"
  "v_exp_f32_e64 v152, v152\n\t"
  "v_mfma_f32_16x16x32_bf16 v[244:247], v[236:239], a[244:247], v[244:247]\n\t"
  "buffer_load_dwordx4 a[244:247], v201, %7, 0 offen offset:1024\n\t"
  "v_mad_u32_u16 v200, v209, s90, v227\n\t"
  "v_mfma_f32_16x16x32_bf16 v[96:99], a[96:99], v[172:175], v[96:99]\n\t"
  "ds_read_b128 a[92:95], v217 offset:34304\n\t"
  "v_mfma_f32_16x16x32_bf16 v[100:103], a[100:103], v[172:175], v[100:103]\n\t"
  "v_exp_f32_e64 v153, v153\n\t"
  "v_mfma_f32_16x16x32_bf16 v[104:107], a[104:107], v[172:175], v[104:107]\n\t"
  "ds_read_b128 a[88:91], v217 offset:33792\n\t"
  "v_cvt_pk_bf16_f32 v188, v152, v153\n\t"
  "v_mfma_f32_16x16x32_bf16 v[108:111], a[108:111], v[172:175], v[108:111]\n\t"
  "v_exp_f32_e64 v154, v154\n\t"
  "v_mfma_f32_16x16x32_bf16 v[112:115], a[112:115], v[172:175], v[112:115]\n\t"
  "v_mfma_f32_16x16x32_bf16 v[116:119], a[116:119], v[172:175], v[116:119]\n\t"
  "s_waitcnt vmcnt(44)\n\t"
  "v_exp_f32_e32 v155, v155\n\t"
  "v_mfma_f32_16x16x32_bf16 v[120:123], a[120:123], v[172:175], v[120:123]\n\t"
  "v_cvt_pk_bf16_f32 v189, v154, v155\n\t"
  "v_mfma_f32_16x16x32_bf16 v[124:127], a[124:127], v[172:175], v[124:127]\n\t"
  "s_waitcnt lgkmcnt(2)\n\t"
  "v_exp_f32_e32 v156, v156\n\t"
  "v_mfma_f32_16x16x32_bf16 v[244:247], v[236:239], a[248:251], v[244:247]\n\t"
  "buffer_load_dwordx4 a[248:251], v200, %7, 0 offen offset:1024\n\t"
  "v_mad_u32_u16 v201, v209, s90, v227 op_sel:[1,0,0,0]\n\t"
  "v_mfma_f32_16x16x32_bf16 v[128:131], a[64:67], a[0:3], 0\n\t"
  "ds_read_b64_tr_b16 a[96:97], v218 offset:24576\n\t"
  "ds_read_b64_tr_b16 a[98:99], v219 offset:24576\n\t"
  "v_mfma_f32_16x16x32_bf16 v[128:131], a[68:71], a[4:7], v[128:131]\n\t"
  "v_exp_f32_e64 v157, v157\n\t"
  "v_mfma_f32_16x16x32_bf16 v[128:131], a[72:75], a[8:11], v[128:131]\n\t"
  "ds_read_b64_tr_b16 a[100:101], v196 offset:24576\n\t"
  "v_cvt_pk_bf16_f32 v190, v156, v157\n\t"
  "v_mfma_f32_16x16x32_bf16 v[128:131], a[76:79], a[12:15], v[128:131]\n\t"
  "v_exp_f32_e64 v158, v158\n\t"
  "v_mfma_f32_16x16x32_bf16 v[132:135], a[80:83], a[0:3], 0\n\t"
  "v_mfma_f32_16x16x32_bf16 v[132:135], a[84:87], a[4:7], v[132:135]\n\t"
  "s_waitcnt lgkmcnt(3)\n\t"
  "v_exp_f32_e32 v159, v159\n\t"
  "v_mfma_f32_16x16x32_bf16 v[132:135], a[88:91], a[8:11], v[132:135]\n\t"
  "ds_read_b64_tr_b16 a[102:103], v197 offset:24576\n\t"
  "v_cvt_pk_bf16_f32 v191, v158, v159\n\t"
  "v_mfma_f32_16x16x32_bf16 v[132:135], a[92:95], a[12:15], v[132:135]\n\t"
  "v_exp_f32_e64 v128, v128\n\t"
  "v_mfma_f32_16x16x32_bf16 v[244:247], v[236:239], a[252:255], v[244:247]\n\t"
  "buffer_load_dwordx4 a[252:255], v201, %7, 0 offen offset:1024\n\t"
  "v_mad_u32_u16 v200, v210, s90, v227\n\t"
  "v_mfma_f32_16x16x32_bf16 v[136:139], a[64:67], a[16:19], 0\n\t"
  "ds_read_b64_tr_b16 a[104:105], v218 offset:25088\n\t"
  "ds_read_b64_tr_b16 a[106:107], v219 offset:25088\n\t"
  "v_mfma_f32_16x16x32_bf16 v[136:139], a[68:71], a[20:23], v[136:139]\n\t"
  "v_exp_f32_e64 v129, v129\n\t"
  "v_mfma_f32_16x16x32_bf16 v[136:139], a[72:75], a[24:27], v[136:139]\n\t"
  "ds_read_b64_tr_b16 a[108:109], v196 offset:25088\n\t"
  "v_cvt_pk_bf16_f32 v160, v128, v129\n\t"
  "v_mfma_f32_16x16x32_bf16 v[136:139], a[76:79], a[28:31], v[136:139]\n\t"
  "v_exp_f32_e64 v130, v130\n\t"
  "v_mfma_f32_16x16x32_bf16 v[140:143], a[80:83], a[16:19], 0\n\t"
  "v_mfma_f32_16x16x32_bf16 v[140:143], a[84:87], a[20:23], v[140:143]\n\t"
  "v_exp_f32_e64 v131, v131\n\t"
  "v_mfma_f32_16x16x32_bf16 v[140:143], a[88:91], a[24:27], v[140:143]\n\t"
  "ds_read_b64_tr_b16 a[110:111], v197 offset:25088\n\t"
  "v_cvt_pk_bf16_f32 v161, v130, v131\n\t"
  "v_mfma_f32_16x16x32_bf16 v[140:143], a[92:95], a[28:31], v[140:143]\n\t"
  "s_waitcnt vmcnt(44)\n\t"
  "v_exp_f32_e32 v132, v132\n\t"
  "v_mfma_f32_16x16x32_bf16 v[248:251], v[232:235], a[128:131], 0\n\t"
  "buffer_load_dwordx4 a[128:131], v200, %7, 0 offen offset:1024\n\t"
  "v_mad_u32_u16 v201, v210, s90, v227 op_sel:[1,0,0,0]\n\t"
  "v_mfma_f32_16x16x32_bf16 v[144:147], a[64:67], a[32:35], 0\n\t"
  "ds_read_b64_tr_b16 a[112:113], v218 offset:25600\n\t"
  "ds_read_b64_tr_b16 a[114:115], v219 offset:25600\n\t"
  "v_mfma_f32_16x16x32_bf16 v[144:147], a[68:71], a[36:39], v[144:147]\n\t"
  "v_exp_f32_e64 v133, v133\n\t"
  "v_mfma_f32_16x16x32_bf16 v[144:147], a[72:75], a[40:43], v[144:147]\n\t"
  "ds_read_b64_tr_b16 a[116:117], v196 offset:25600\n\t"
  "v_cvt_pk_bf16_f32 v162, v132, v133\n\t"
  "v_mfma_f32_16x16x32_bf16 v[144:147], a[76:79], a[44:47], v[144:147]\n\t"
  "v_exp_f32_e64 v134, v134\n\t"
  "v_mfma_f32_16x16x32_bf16 v[148:151], a[80:83], a[32:35], 0\n\t"
  "v_mfma_f32_16x16x32_bf16 v[148:151], a[84:87], a[36:39], v[148:151]\n\t"
  "v_exp_f32_e64 v135, v135\n\t"
  "v_mfma_f32_16x16x32_bf16 v[148:151], a[88:91], a[40:43], v[148:151]\n\t"
  "ds_read_b64_tr_b16 a[118:119], v197 offset:25600\n\t"
  "v_cvt_pk_bf16_f32 v163, v134, v135\n\t"
  "v_mfma_f32_16x16x32_bf16 v[148:151], a[92:95], a[44:47], v[148:151]\n\t"
  "v_exp_f32_e64 v136, v136\n\t"
  "v_mfma_f32_16x16x32_bf16 v[248:251], v[232:235], a[132:135], v[248:251]\n\t"
  "buffer_load_dwordx4 a[132:135], v201, %7, 0 offen offset:1024\n\t"
  "v_mad_u32_u16 v200, v211, s90, v227\n\t"
  "v_mfma_f32_16x16x32_bf16 v[152:155], a[64:67], a[48:51], 0\n\t"
  "ds_read_b64_tr_b16 a[120:121], v218 offset:26112\n\t"
  "ds_read_b64_tr_b16 a[122:123], v219 offset:26112\n\t"
  "v_mfma_f32_16x16x32_bf16 v[152:155], a[68:71], a[52:55], v[152:155]\n\t"
  "v_exp_f32_e64 v137, v137\n\t"
  "v_mfma_f32_16x16x32_bf16 v[152:155], a[72:75], a[56:59], v[152:155]\n\t"
  "ds_read_b64_tr_b16 a[124:125], v196 offset:26112\n\t"
  "v_cvt_pk_bf16_f32 v164, v136, v137\n\t"
  "v_mfma_f32_16x16x32_bf16 v[152:155], a[76:79], a[60:63], v[152:155]\n\t"
  "v_exp_f32_e64 v138, v138\n\t"
  "v_mfma_f32_16x16x32_bf16 v[156:159], a[80:83], a[48:51], 0\n\t"
  "v_mfma_f32_16x16x32_bf16 v[156:159], a[84:87], a[52:55], v[156:159]\n\t"
  "s_waitcnt vmcnt(44)\n\t"
  "v_exp_f32_e32 v139, v139\n\t"
  "v_mfma_f32_16x16x32_bf16 v[156:159], a[88:91], a[56:59], v[156:159]\n\t"
  "ds_read_b64_tr_b16 a[126:127], v197 offset:26112\n\t"
  "v_cvt_pk_bf16_f32 v165, v138, v139\n\t"
  "v_mfma_f32_16x16x32_bf16 v[156:159], a[92:95], a[60:63], v[156:159]\n\t"
  "s_waitcnt lgkmcnt(2)\n\t"
  "v_exp_f32_e32 v140, v140\n\t"
  "v_mfma_f32_16x16x32_bf16 v[248:251], v[232:235], a[136:139], v[248:251]\n\t"
  "buffer_load_dwordx4 a[136:139], v200, %7, 0 offen offset:1024\n\t"
  "v_mad_u32_u16 v201, v211, s90, v227 op_sel:[1,0,0,0]\n\t"
  "v_mfma_f32_16x16x32_bf16 v[0:3], a[96:99], v[176:179], v[0:3]\n\t"
  "v_mfma_f32_16x16x32_bf16 v[4:7], a[100:103], v[176:179], v[4:7]\n\t"
  "v_exp_f32_e64 v141, v141\n\t"
  "v_mfma_f32_16x16x32_bf16 v[8:11], a[104:107], v[176:179], v[8:11]\n\t"
  "ds_read_b128 a[64:67], v216 offset:40960\n\t"
  "v_cvt_pk_bf16_f32 v166, v140, v141\n\t"
  "v_mfma_f32_16x16x32_bf16 v[12:15], a[108:111], v[176:179], v[12:15]\n\t"
  "v_exp_f32_e64 v142, v142\n\t"
  "v_mfma_f32_16x16x32_bf16 v[16:19], a[112:115], v[176:179], v[16:19]\n\t"
  "v_mfma_f32_16x16x32_bf16 v[20:23], a[116:119], v[176:179], v[20:23]\n\t"
  "s_waitcnt lgkmcnt(1)\n\t"
  "v_exp_f32_e32 v143, v143\n\t"
  "v_mfma_f32_16x16x32_bf16 v[24:27], a[120:123], v[176:179], v[24:27]\n\t"
  "ds_read_b128 a[68:71], v216 offset:41472\n\t"
  "v_cvt_pk_bf16_f32 v167, v142, v143\n\t"
  "v_mfma_f32_16x16x32_bf16 v[28:31], a[124:127], v[176:179], v[28:31]\n\t"
  "v_exp_f32_e64 v144, v144\n\t"
  "v_mfma_f32_16x16x32_bf16 v[248:251], v[232:235], a[140:143], v[248:251]\n\t"
  "buffer_load_dwordx4 a[140:143], v201, %7, 0 offen offset:1024\n\t"
  "v_mad_u32_u16 v200, v212, s90, v227\n\t"
  "v_mfma_f32_16x16x32_bf16 v[32:35], a[96:99], v[180:183], v[32:35]\n\t"
  "v_mfma_f32_16x16x32_bf16 v[36:39], a[100:103], v[180:183], v[36:39]\n\t"
  "v_exp_f32_e64 v145, v145\n\t"
  "v_mfma_f32_16x16x32_bf16 v[40:43], a[104:107], v[180:183], v[40:43]\n\t"
  "ds_read_b128 a[72:75], v216 offset:41984\n\t"
  "v_cvt_pk_bf16_f32 v168, v144, v145\n\t"
  "v_mfma_f32_16x16x32_bf16 v[44:47], a[108:111], v[180:183], v[44:47]\n\t"
  "v_exp_f32_e64 v146, v146\n\t"
  "v_mfma_f32_16x16x32_bf16 v[48:51], a[112:115], v[180:183], v[48:51]\n\t"
  "v_mfma_f32_16x16x32_bf16 v[52:55], a[116:119], v[180:183], v[52:55]\n\t"
  "v_exp_f32_e64 v147, v147\n\t"
  "v_mfma_f32_16x16x32_bf16 v[56:59], a[120:123], v[180:183], v[56:59]\n\t"
  "ds_read_b128 a[76:79], v216 offset:42496\n\t"
  "v_cvt_pk_bf16_f32 v169, v146, v147\n\t"
  "v_mfma_f32_16x16x32_bf16 v[60:63], a[124:127], v[180:183], v[60:63]\n\t"
  "s_waitcnt vmcnt(42)\n\t"
  "v_exp_f32_e32 v148, v148\n\t"
  "v_mfma_f32_16x16x32_bf16 v[248:251], v[232:235], a[144:147], v[248:251]\n\t"
  "buffer_load_dwordx4 a[144:147], v200, %7, 0 offen offset:1024\n\t"
  "v_mad_u32_u16 v201, v212, s90, v227 op_sel:[1,0,0,0]\n\t"
  "v_mfma_f32_16x16x32_bf16 v[64:67], a[96:99], v[184:187], v[64:67]\n\t"
  "v_mfma_f32_16x16x32_bf16 v[68:71], a[100:103], v[184:187], v[68:71]\n\t"
  "v_exp_f32_e64 v149, v149\n\t"
  "v_mfma_f32_16x16x32_bf16 v[72:75], a[104:107], v[184:187], v[72:75]\n\t"
  "ds_read_b128 a[80:83], v217 offset:40960\n\t"
  "v_cvt_pk_bf16_f32 v170, v148, v149\n\t"
  "v_mfma_f32_16x16x32_bf16 v[76:79], a[108:111], v[184:187], v[76:79]\n\t"
  "v_exp_f32_e64 v150, v150\n\t"
  "v_mfma_f32_16x16x32_bf16 v[80:83], a[112:115], v[184:187], v[80:83]\n\t"
  "v_mfma_f32_16x16x32_bf16 v[84:87], a[116:119], v[184:187], v[84:87]\n\t"
  "v_exp_f32_e64 v151, v151\n\t"
  "v_mfma_f32_16x16x32_bf16 v[88:91], a[120:123], v[184:187], v[88:91]\n\t"
  "ds_read_b128 a[84:87], v217 offset:41472\n\t"
  "v_cvt_pk_bf16_f32 v171, v150, v151\n\t"
  "v_mfma_f32_16x16x32_bf16 v[92:95], a[124:127], v[184:187], v[92:95]\n\t"
  "v_exp_f32_e64 v152, v152\n\t"
  "v_mfma_f32_16x16x32_bf16 v[248:251], v[232:235], a[148:151], v[248:251]\n\t"
  "buffer_load_dwordx4 a[148:151], v201, %7, 0 offen offset:1024\n\t"
  "v_mad_u32_u16 v200, v213, s90, v227\n\t"
  "v_mfma_f32_16x16x32_bf16 v[96:99], a[96:99], v[188:191], v[96:99]\n\t"
  "ds_read_b128 a[92:95], v217 offset:42496\n\t"
  "v_mfma_f32_16x16x32_bf16 v[100:103], a[100:103], v[188:191], v[100:103]\n\t"
  "v_exp_f32_e64 v153, v153\n\t"
  "v_mfma_f32_16x16x32_bf16 v[104:107], a[104:107], v[188:191], v[104:107]\n\t"
  "ds_read_b128 a[88:91], v217 offset:41984\n\t"
  "v_cvt_pk_bf16_f32 v172, v152, v153\n\t"
  "v_mfma_f32_16x16x32_bf16 v[108:111], a[108:111], v[188:191], v[108:111]\n\t"
  "v_exp_f32_e64 v154, v154\n\t"
  "v_mfma_f32_16x16x32_bf16 v[112:115], a[112:115], v[188:191], v[112:115]\n\t"
  "v_mfma_f32_16x16x32_bf16 v[116:119], a[116:119], v[188:191], v[116:119]\n\t"
  "s_waitcnt vmcnt(40)\n\t"
  "v_exp_f32_e32 v155, v155\n\t"
  "v_mfma_f32_16x16x32_bf16 v[120:123], a[120:123], v[188:191], v[120:123]\n\t"
  "v_cvt_pk_bf16_f32 v173, v154, v155\n\t"
  "v_mfma_f32_16x16x32_bf16 v[124:127], a[124:127], v[188:191], v[124:127]\n\t"
  "s_waitcnt lgkmcnt(2)\n\t"
  "v_exp_f32_e32 v156, v156\n\t"
  "v_mfma_f32_16x16x32_bf16 v[248:251], v[232:235], a[152:155], v[248:251]\n\t"
  "buffer_load_dwordx4 a[152:155], v200, %7, 0 offen offset:1024\n\t"
  "v_mad_u32_u16 v201, v213, s90, v227 op_sel:[1,0,0,0]\n\t"
  "v_mfma_f32_16x16x32_bf16 v[128:131], a[64:67], a[0:3], 0\n\t"
  "ds_read_b64_tr_b16 a[96:97], v218 offset:32768\n\t"
  "ds_read_b64_tr_b16 a[98:99], v219 offset:32768\n\t"
  "v_mfma_f32_16x16x32_bf16 v[128:131], a[68:71], a[4:7], v[128:131]\n\t"
  "v_exp_f32_e64 v157, v157\n\t"
  "v_mfma_f32_16x16x32_bf16 v[128:131], a[72:75], a[8:11], v[128:131]\n\t"
  "ds_read_b64_tr_b16 a[100:101], v196 offset:32768\n\t"
  "v_cvt_pk_bf16_f32 v174, v156, v157\n\t"
  "v_mfma_f32_16x16x32_bf16 v[128:131], a[76:79], a[12:15], v[128:131]\n\t"
  "v_exp_f32_e64 v158, v158\n\t"
  "v_mfma_f32_16x16x32_bf16 v[132:135], a[80:83], a[0:3], 0\n\t"
  "v_mfma_f32_16x16x32_bf16 v[132:135], a[84:87], a[4:7], v[132:135]\n\t"
  "s_waitcnt lgkmcnt(3)\n\t"
  "v_exp_f32_e32 v159, v159\n\t"
  "v_mfma_f32_16x16x32_bf16 v[132:135], a[88:91], a[8:11], v[132:135]\n\t"
  "ds_read_b64_tr_b16 a[102:103], v197 offset:32768\n\t"
  "v_cvt_pk_bf16_f32 v175, v158, v159\n\t"
  "v_mfma_f32_16x16x32_bf16 v[132:135], a[92:95], a[12:15], v[132:135]\n\t"
  "v_exp_f32_e64 v128, v128\n\t"
  "v_mfma_f32_16x16x32_bf16 v[248:251], v[232:235], a[156:159], v[248:251]\n\t"
  "buffer_load_dwordx4 a[156:159], v201, %7, 0 offen offset:1024\n\t"
  "v_mfma_f32_16x16x32_bf16 v[136:139], a[64:67], a[16:19], 0\n\t"
  "ds_read_b64_tr_b16 a[104:105], v218 offset:33280\n\t"
  "ds_read_b64_tr_b16 a[106:107], v219 offset:33280\n\t"
  "v_mfma_f32_16x16x32_bf16 v[136:139], a[68:71], a[20:23], v[136:139]\n\t"
  "v_exp_f32_e64 v129, v129\n\t"
  "v_mfma_f32_16x16x32_bf16 v[136:139], a[72:75], a[24:27], v[136:139]\n\t"
  "ds_read_b64_tr_b16 a[108:109], v196 offset:33280\n\t"
  "v_cvt_pk_bf16_f32 v176, v128, v129\n\t"
  "v_mfma_f32_16x16x32_bf16 v[136:139], a[76:79], a[28:31], v[136:139]\n\t"
  "v_exp_f32_e64 v130, v130\n\t"
  "v_mfma_f32_16x16x32_bf16 v[140:143], a[80:83], a[16:19], 0\n\t"
  "v_mfma_f32_16x16x32_bf16 v[140:143], a[84:87], a[20:23], v[140:143]\n\t"
  "v_exp_f32_e64 v131, v131\n\t"
  "v_mfma_f32_16x16x32_bf16 v[140:143], a[88:91], a[24:27], v[140:143]\n\t"
  "ds_read_b64_tr_b16 a[110:111], v197 offset:33280\n\t"
  "v_cvt_pk_bf16_f32 v177, v130, v131\n\t"
  "v_mfma_f32_16x16x32_bf16 v[140:143], a[92:95], a[28:31], v[140:143]\n\t"
  "v_exp_f32_e64 v132, v132\n\t"
  "v_mfma_f32_16x16x32_bf16 v[144:147], a[64:67], a[32:35], 0\n\t"
  "ds_read_b64_tr_b16 a[112:113], v218 offset:33792\n\t"
  "ds_read_b64_tr_b16 a[114:115], v219 offset:33792\n\t"
  "v_mfma_f32_16x16x32_bf16 v[144:147], a[68:71], a[36:39], v[144:147]\n\t"
  "v_exp_f32_e64 v133, v133\n\t"
  "v_mfma_f32_16x16x32_bf16 v[144:147], a[72:75], a[40:43], v[144:147]\n\t"
  "ds_read_b64_tr_b16 a[116:117], v196 offset:33792\n\t"
  "v_cvt_pk_bf16_f32 v178, v132, v133\n\t"
  "v_mfma_f32_16x16x32_bf16 v[144:147], a[76:79], a[44:47], v[144:147]\n\t"
  "v_exp_f32_e64 v134, v134\n\t"
  "v_mfma_f32_16x16x32_bf16 v[148:151], a[80:83], a[32:35], 0\n\t"
  "buffer_load_dwordx4 a[0:3], v230, %9, 0 offen offset:0\n\t"
  "v_mfma_f32_16x16x32_bf16 v[148:151], a[84:87], a[36:39], v[148:151]\n\t"
  "v_exp_f32_e64 v135, v135\n\t"
  "v_mfma_f32_16x16x32_bf16 v[148:151], a[88:91], a[40:43], v[148:151]\n\t"
  "ds_read_b64_tr_b16 a[118:119], v197 offset:33792\n\t"
  "v_cvt_pk_bf16_f32 v179, v134, v135\n\t"
  "v_mfma_f32_16x16x32_bf16 v[148:151], a[92:95], a[44:47], v[148:151]\n\t"
  "v_exp_f32_e64 v136, v136\n\t"
  "v_mfma_f32_16x16x32_bf16 v[152:155], a[64:67], a[48:51], 0\n\t"
  "ds_read_b64_tr_b16 a[120:121], v218 offset:34304\n\t"
  "ds_read_b64_tr_b16 a[122:123], v219 offset:34304\n\t"
  "v_mfma_f32_16x16x32_bf16 v[152:155], a[68:71], a[52:55], v[152:155]\n\t"
  "v_exp_f32_e64 v137, v137\n\t"
  "v_mfma_f32_16x16x32_bf16 v[152:155], a[72:75], a[56:59], v[152:155]\n\t"
  "ds_read_b64_tr_b16 a[124:125], v196 offset:34304\n\t"
  "v_cvt_pk_bf16_f32 v180, v136, v137\n\t"
  "v_mfma_f32_16x16x32_bf16 v[152:155], a[76:79], a[60:63], v[152:155]\n\t"
  "v_exp_f32_e64 v138, v138\n\t"
  "v_mfma_f32_16x16x32_bf16 v[156:159], a[80:83], a[48:51], 0\n\t"
  "buffer_load_dwordx4 a[4:7], v230, %9, 0 offen offset:64\n\t"
  "v_mfma_f32_16x16x32_bf16 v[156:159], a[84:87], a[52:55], v[156:159]\n\t"
  "v_exp_f32_e64 v139, v139\n\t"
  "v_mfma_f32_16x16x32_bf16 v[156:159], a[88:91], a[56:59], v[156:159]\n\t"
  "ds_read_b64_tr_b16 a[126:127], v197 offset:34304\n\t"
  "v_cvt_pk_bf16_f32 v181, v138, v139\n\t"
  "v_mfma_f32_16x16x32_bf16 v[156:159], a[92:95], a[60:63], v[156:159]\n\t"
  "s_waitcnt lgkmcnt(2)\n\t"
  "v_exp_f32_e32 v140, v140\n\t"
  "v_mfma_f32_16x16x32_bf16 v[0:3], a[96:99], v[160:163], v[0:3]\n\t"
  "ds_read_b64_tr_b16 a[64:65], v218 offset:40960\n\t"
  "ds_read_b64_tr_b16 a[66:67], v219 offset:40960\n\t"
  "v_mfma_f32_16x16x32_bf16 v[4:7], a[100:103], v[160:163], v[4:7]\n\t"
  "v_exp_f32_e64 v141, v141\n\t"
  "v_mfma_f32_16x16x32_bf16 v[8:11], a[104:107], v[160:163], v[8:11]\n\t"
  "ds_read_b64_tr_b16 a[68:69], v196 offset:40960\n\t"
  "v_cvt_pk_bf16_f32 v182, v140, v141\n\t"
  "v_mfma_f32_16x16x32_bf16 v[12:15], a[108:111], v[160:163], v[12:15]\n\t"
  "v_exp_f32_e64 v142, v142\n\t"
  "v_mfma_f32_16x16x32_bf16 v[16:19], a[112:115], v[160:163], v[16:19]\n\t"
  "buffer_load_dwordx4 a[8:11], v230, %9, 0 offen offset:128\n\t"
  "buffer_load_dwordx4 a[12:15], v230, %9, 0 offen offset:192\n\t"
  "v_mfma_f32_16x16x32_bf16 v[20:23], a[116:119], v[160:163], v[20:23]\n\t"
  "s_waitcnt lgkmcnt(3)\n\t"
  "v_exp_f32_e32 v143, v143\n\t"
  "v_mfma_f32_16x16x32_bf16 v[24:27], a[120:123], v[160:163], v[24:27]\n\t"
  "ds_read_b64_tr_b16 a[70:71], v197 offset:40960\n\t"
  "v_cvt_pk_bf16_f32 v183, v142, v143\n\t"
  "v_mfma_f32_16x16x32_bf16 v[28:31], a[124:127], v[160:163], v[28:31]\n\t"
  "v_exp_f32_e64 v144, v144\n\t"
  "v_mfma_f32_16x16x32_bf16 v[32:35], a[96:99], v[164:167], v[32:35]\n\t"
  "ds_read_b64_tr_b16 a[72:73], v218 offset:41472\n\t"
  "ds_read_b64_tr_b16 a[74:75], v219 offset:41472\n\t"
  "v_mfma_f32_16x16x32_bf16 v[36:39], a[100:103], v[164:167], v[36:39]\n\t"
  "v_exp_f32_e64 v145, v145\n\t"
  "v_mfma_f32_16x16x32_bf16 v[40:43], a[104:107], v[164:167], v[40:43]\n\t"
  "ds_read_b64_tr_b16 a[76:77], v196 offset:41472\n\t"
  "v_cvt_pk_bf16_f32 v184, v144, v145\n\t"
  "v_mfma_f32_16x16x32_bf16 v[44:47], a[108:111], v[164:167], v[44:47]\n\t"
  "v_exp_f32_e64 v146, v146\n\t"
  "v_mfma_f32_16x16x32_bf16 v[48:51], a[112:115], v[164:167], v[48:51]\n\t"
  "buffer_load_dwordx4 a[16:19], v231, %8, 0 offen offset:0\n\t"
  "buffer_load_dwordx4 a[20:23], v231, %8, 0 offen offset:1024\n\t"
  "v_mfma_f32_16x16x32_bf16 v[52:55], a[116:119], v[164:167], v[52:55]\n\t"
  "v_exp_f32_e64 v147, v147\n\t"
  "v_mfma_f32_16x16x32_bf16 v[56:59], a[120:123], v[164:167], v[56:59]\n\t"
  "ds_read_b64_tr_b16 a[78:79], v197 offset:41472\n\t"
  "v_cvt_pk_bf16_f32 v185, v146, v147\n\t"
  "v_mfma_f32_16x16x32_bf16 v[60:63], a[124:127], v[164:167], v[60:63]\n\t"
  "v_exp_f32_e64 v148, v148\n\t"
  "v_mfma_f32_16x16x32_bf16 v[64:67], a[96:99], v[168:171], v[64:67]\n\t"
  "ds_read_b64_tr_b16 a[80:81], v218 offset:41984\n\t"
  "ds_read_b64_tr_b16 a[82:83], v219 offset:41984\n\t"
  "v_mfma_f32_16x16x32_bf16 v[68:71], a[100:103], v[168:171], v[68:71]\n\t"
  "v_exp_f32_e64 v149, v149\n\t"
  "v_mfma_f32_16x16x32_bf16 v[72:75], a[104:107], v[168:171], v[72:75]\n\t"
  "ds_read_b64_tr_b16 a[84:85], v196 offset:41984\n\t"
  "v_cvt_pk_bf16_f32 v186, v148, v149\n\t"
  "v_mfma_f32_16x16x32_bf16 v[76:79], a[108:111], v[168:171], v[76:79]\n\t"
  "v_exp_f32_e64 v150, v150\n\t"
  "v_mfma_f32_16x16x32_bf16 v[80:83], a[112:115], v[168:171], v[80:83]\n\t"
  "buffer_load_dwordx4 a[24:27], v231, %8, 0 offen offset:2048\n\t"
  "buffer_load_dwordx4 a[28:31], v231, %8, 0 offen offset:3072\n\t"
  "v_mfma_f32_16x16x32_bf16 v[84:87], a[116:119], v[168:171], v[84:87]\n\t"
  "v_exp_f32_e64 v151, v151\n\t"
  "v_mfma_f32_16x16x32_bf16 v[88:91], a[120:123], v[168:171], v[88:91]\n\t"
  "ds_read_b64_tr_b16 a[86:87], v197 offset:41984\n\t"
  "v_cvt_pk_bf16_f32 v187, v150, v151\n\t"
  "v_mfma_f32_16x16x32_bf16 v[92:95], a[124:127], v[168:171], v[92:95]\n\t"
  "v_exp_f32_e64 v152, v152\n\t"
  "v_mfma_f32_16x16x32_bf16 v[96:99], a[96:99], v[172:175], v[96:99]\n\t"
  "ds_read_b64_tr_b16 a[88:89], v218 offset:42496\n\t"
  "ds_read_b64_tr_b16 a[90:91], v219 offset:42496\n\t"
  "v_mfma_f32_16x16x32_bf16 v[100:103], a[100:103], v[172:175], v[100:103]\n\t"
  "v_exp_f32_e64 v153, v153\n\t"
  "v_mfma_f32_16x16x32_bf16 v[104:107], a[104:107], v[172:175], v[104:107]\n\t"
  "ds_read_b64_tr_b16 a[92:93], v196 offset:42496\n\t"
  "v_cvt_pk_bf16_f32 v188, v152, v153\n\t"
  "v_mfma_f32_16x16x32_bf16 v[108:111], a[108:111], v[172:175], v[108:111]\n\t"
  "v_exp_f32_e64 v154, v154\n\t"
  "v_mfma_f32_16x16x32_bf16 v[112:115], a[112:115], v[172:175], v[112:115]\n\t"
  "s_waitcnt vmcnt(23)\n\t"
  "s_barrier\n\t"
  "v_mfma_f32_16x16x32_bf16 v[116:119], a[116:119], v[172:175], v[116:119]\n\t"
  "v_exp_f32_e64 v155, v155\n\t"
  "v_mfma_f32_16x16x32_bf16 v[120:123], a[120:123], v[172:175], v[120:123]\n\t"
  "ds_read_b64_tr_b16 a[94:95], v197 offset:42496\n\t"
  "v_cvt_pk_bf16_f32 v189, v154, v155\n\t"
  "v_mfma_f32_16x16x32_bf16 v[124:127], a[124:127], v[172:175], v[124:127]\n\t"
  "s_waitcnt lgkmcnt(2)\n\t"
  "v_exp_f32_e32 v156, v156\n\t"
  "v_and_b32_e32 v228, 0x7f, v214\n\t"
  "v_cmp_lt_u32_e64 s[74:75], 64, v228\n\t"
  "v_cmp_lt_u32_e32 vcc, 48, v228\n\t"
  "s_and_b32 s72, s74, 0x1ff0\n\t"
  "s_add_u32 s72, s72, %0\n\t"
  "s_cmp_gt_u32 s72, 2\n\t"
  "s_cbranch_scc1 LSLOW%=\n\t"
  "s_lshr_b32 s87, vcc_lo, 1\n\t"
  "s_and_b32 s87, s87, vcc_lo\n\t"
  "s_and_b32 s87, s87, 0x550\n\t"
  "v_mfma_f32_16x16x32_bf16 v[0:3], a[64:67], v[176:179], v[0:3]\n\t"
  "s_mov_b32 s63, s55\n\t"
  "v_exp_f32_e32 v157, v157\n\t"
  "v_mfma_f32_16x16x32_bf16 v[4:7], a[68:71], v[176:179], v[4:7]\n\t"
  "v_cvt_pk_bf16_f32 v190, v156, v157\n\t"
  "v_mfma_f32_16x16x32_bf16 v[8:11], a[72:75], v[176:179], v[8:11]\n\t"
  "v_exp_f32_e32 v158, v158\n\t"
  "v_mfma_f32_16x16x32_bf16 v[12:15], a[76:79], v[176:179], v[12:15]\n\t"
  "s_waitcnt lgkmcnt(0)\n\t"
  "v_exp_f32_e32 v159, v159\n\t"
  "v_mfma_f32_16x16x32_bf16 v[16:19], a[80:83], v[176:179], v[16:19]\n\t"
  "v_cvt_pk_bf16_f32 v191, v158, v159\n\t"
  "v_mfma_f32_16x16x32_bf16 v[20:23], a[84:87], v[176:179], v[20:23]\n\t"
  "v_cvt_pk_bf16_f32 v0, v0, v1\n\t"
  "v_cvt_pk_bf16_f32 v1, v2, v3\n\t"
  "v_mfma_f32_16x16x32_bf16 v[24:27], a[88:91], v[176:179], v[24:27]\n\t"
  "v_cvt_pk_bf16_f32 v2, v4, v5\n\t"
  "v_cvt_pk_bf16_f32 v3, v6, v7\n\t"
  "v_mfma_f32_16x16x32_bf16 v[28:31], a[92:95], v[176:179], v[28:31]\n\t"
  "buffer_store_dwordx4 v[0:3], v220, %8, s63 offen offset:0 sc1\n\t"
  "v_cvt_pk_bf16_f32 v8, v8, v9\n\t"
  "v_cvt_pk_bf16_f32 v9, v10, v11\n\t"
  "v_mfma_f32_16x16x32_bf16 v[32:35], a[64:67], v[180:183], v[32:35]\n\t"
  "v_cvt_pk_bf16_f32 v10, v12, v13\n\t"
  "v_cvt_pk_bf16_f32 v11, v14, v15\n\t"
  "v_mfma_f32_16x16x32_bf16 v[36:39], a[68:71], v[180:183], v[36:39]\n\t"
  "v_cvt_pk_bf16_f32 v16, v16, v17\n\t"
  "v_cvt_pk_bf16_f32 v17, v18, v19\n\t"
  "v_mfma_f32_16x16x32_bf16 v[40:43], a[72:75], v[180:183], v[40:43]\n\t"
  "buffer_store_dwordx4 v[8:11], v220, %8, s63 offen offset:1024 sc1\n\t"
  "v_cvt_pk_bf16_f32 v18, v20, v21\n\t"
  "v_cvt_pk_bf16_f32 v19, v22, v23\n\t"
  "v_mfma_f32_16x16x32_bf16 v[44:47], a[76:79], v[180:183], v[44:47]\n\t"
  "v_cvt_pk_bf16_f32 v24, v24, v25\n\t"
  "v_cvt_pk_bf16_f32 v25, v26, v27\n\t"
  "v_mfma_f32_16x16x32_bf16 v[48:51], a[80:83], v[180:183], v[48:51]\n\t"
  "v_cvt_pk_bf16_f32 v26, v28, v29\n\t"
  "v_cvt_pk_bf16_f32 v27, v30, v31\n\t"
  "v_mfma_f32_16x16x32_bf16 v[52:55], a[84:87], v[180:183], v[52:55]\n\t"
  "v_cvt_pk_bf16_f32 v32, v32, v33\n\t"
  "v_cvt_pk_bf16_f32 v33, v34, v35\n\t"
  "v_mfma_f32_16x16x32_bf16 v[56:59], a[88:91], v[180:183], v[56:59]\n\t"
  "v_cvt_pk_bf16_f32 v34, v36, v37\n\t"
  "v_cvt_pk_bf16_f32 v35, v38, v39\n\t"
  "v_mfma_f32_16x16x32_bf16 v[60:63], a[92:95], v[180:183], v[60:63]\n\t"
  "buffer_store_dwordx4 v[16:19], v220, %8, s63 offen offset:2048 sc1\n\t"
  "v_cvt_pk_bf16_f32 v40, v40, v41\n\t"
  "v_cvt_pk_bf16_f32 v41, v42, v43\n\t"
  "v_mfma_f32_16x16x32_bf16 v[64:67], a[64:67], v[184:187], v[64:67]\n\t"
  "v_cvt_pk_bf16_f32 v42, v44, v45\n\t"
  "v_cvt_pk_bf16_f32 v43, v46, v47\n\t"
  "v_mfma_f32_16x16x32_bf16 v[68:71], a[68:71], v[184:187], v[68:71]\n\t"
  "v_cvt_pk_bf16_f32 v48, v48, v49\n\t"
  "v_cvt_pk_bf16_f32 v49, v50, v51\n\t"
  "v_mfma_f32_16x16x32_bf16 v[72:75], a[72:75], v[184:187], v[72:75]\n\t"
  "v_cvt_pk_bf16_f32 v50, v52, v53\n\t"
  "v_cvt_pk_bf16_f32 v51, v54, v55\n\t"
  "v_mfma_f32_16x16x32_bf16 v[76:79], a[76:79], v[184:187], v[76:79]\n\t"
  "v_cvt_pk_bf16_f32 v56, v56, v57\n\t"
  "v_cvt_pk_bf16_f32 v57, v58, v59\n\t"
  "v_mfma_f32_16x16x32_bf16 v[80:83], a[80:83], v[184:187], v[80:83]\n\t"
  "buffer_store_dwordx4 v[24:27], v220, %8, s63 offen offset:3072 sc1\n\t"
  "v_cvt_pk_bf16_f32 v58, v60, v61\n\t"
  "v_cvt_pk_bf16_f32 v59, v62, v63\n\t"
  "v_mfma_f32_16x16x32_bf16 v[84:87], a[84:87], v[184:187], v[84:87]\n\t"
  "s_add_u32 s63, s63, 0x1000\n\t"
  "v_cvt_pk_bf16_f32 v64, v64, v65\n\t"
  "v_cvt_pk_bf16_f32 v65, v66, v67\n\t"
  "v_mfma_f32_16x16x32_bf16 v[88:91], a[88:91], v[184:187], v[88:91]\n\t"
  "v_cvt_pk_bf16_f32 v66, v68, v69\n\t"
  "v_cvt_pk_bf16_f32 v67, v70, v71\n\t"
  "v_mfma_f32_16x16x32_bf16 v[92:95], a[92:95], v[184:187], v[92:95]\n\t"
  "v_cvt_pk_bf16_f32 v72, v72, v73\n\t"
  "v_cvt_pk_bf16_f32 v73, v74, v75\n\t"
  "v_mfma_f32_16x16x32_bf16 v[96:99], a[64:67], v[188:191], v[96:99]\n\t"
  "buffer_store_dwordx4 v[32:35], v221, %8, s63 offen offset:0 sc1\n\t"
  "v_cvt_pk_bf16_f32 v74, v76, v77\n\t"
  "v_cvt_pk_bf16_f32 v75, v78, v79\n\t"
  "v_mfma_f32_16x16x32_bf16 v[100:103], a[68:71], v[188:191], v[100:103]\n\t"
  "v_cvt_pk_bf16_f32 v80, v80, v81\n\t"
  "v_cvt_pk_bf16_f32 v81, v82, v83\n\t"
  "v_mfma_f32_16x16x32_bf16 v[104:107], a[72:75], v[188:191], v[104:107]\n\t"
  "v_cvt_pk_bf16_f32 v82, v84, v85\n\t"
  "v_cvt_pk_bf16_f32 v83, v86, v87\n\t"
  "v_mfma_f32_16x16x32_bf16 v[108:111], a[76:79], v[188:191], v[108:111]\n\t"
  "v_cvt_pk_bf16_f32 v88, v88, v89\n\t"
  "v_cvt_pk_bf16_f32 v89, v90, v91\n\t"
  "v_mfma_f32_16x16x32_bf16 v[112:115], a[80:83], v[188:191], v[112:115]\n\t"
  "v_cvt_pk_bf16_f32 v90, v92, v93\n\t"
  "v_cvt_pk_bf16_f32 v91, v94, v95\n\t"
  "v_mfma_f32_16x16x32_bf16 v[116:119], a[84:87], v[188:191], v[116:119]\n\t"
  "buffer_store_dwordx4 v[40:43], v221, %8, s63 offen offset:1024 sc1\n\t"
  "v_cvt_pk_bf16_f32 v96, v96, v97\n\t"
  "v_cvt_pk_bf16_f32 v97, v98, v99\n\t"
  "v_mfma_f32_16x16x32_bf16 v[120:123], a[88:91], v[188:191], v[120:123]\n\t"
  "v_cvt_pk_bf16_f32 v98, v100, v101\n\t"
  "v_cvt_pk_bf16_f32 v99, v102, v103\n\t"
  "v_mfma_f32_16x16x32_bf16 v[124:127], a[92:95], v[188:191], v[124:127]\n\t"
  "s_waitcnt vmcnt(6)\n\t"
  "v_mfma_f32_16x16x32_bf16 v[248:251], v[236:239], a[160:163], v[248:251]\n\t"
  "v_cvt_pk_bf16_f32 v104, v104, v105\n\t"
  "v_cvt_pk_bf16_f32 v105, v106, v107\n\t"
  "v_mfma_f32_16x16x32_bf16 v[248:251], v[236:239], a[164:167], v[248:251]\n\t"
  "v_cvt_pk_bf16_f32 v106, v108, v109\n\t"
  "v_cvt_pk_bf16_f32 v107, v110, v111\n\t"
  "v_mfma_f32_16x16x32_bf16 v[248:251], v[236:239], a[168:171], v[248:251]\n\t"
  "buffer_store_dwordx4 v[48:51], v221, %8, s63 offen offset:2048 sc1\n\t"
  "v_cvt_pk_bf16_f32 v112, v112, v113\n\t"
  "v_cvt_pk_bf16_f32 v113, v114, v115\n\t"
  "v_mfma_f32_16x16x32_bf16 v[248:251], v[236:239], a[172:175], v[248:251]\n\t"
  "v_cvt_pk_bf16_f32 v114, v116, v117\n\t"
  "v_cvt_pk_bf16_f32 v115, v118, v119\n\t"
  "v_mfma_f32_16x16x32_bf16 v[248:251], v[236:239], a[176:179], v[248:251]\n\t"
  "v_cvt_pk_bf16_f32 v120, v120, v121\n\t"
  "v_cvt_pk_bf16_f32 v121, v122, v123\n\t"
  "v_mfma_f32_16x16x32_bf16 v[248:251], v[236:239], a[180:183], v[248:251]\n\t"
  "v_cvt_pk_bf16_f32 v122, v124, v125\n\t"
  "v_cvt_pk_bf16_f32 v123, v126, v127\n\t"
  "v_mfma_f32_16x16x32_bf16 v[248:251], v[236:239], a[184:187], v[248:251]\n\t"
  "buffer_store_dwordx4 v[56:59], v221, %8, s63 offen offset:3072 sc1\n\t"
  "v_lshrrev_b32_e32 v207, 4, v224\n\t"
  "v_and_b32_e32 v207, 48, v207\n\t"
  "v_mfma_f32_16x16x32_bf16 v[248:251], v[236:239], a[188:191], v[248:251]\n\t"
  "s_add_u32 s63, s63, 0x1000\n\t"
  "v_lshl_add_u32 v212, v207, 6, v227\n\t"
  "v_add_u32_e32 v212, s40, v212\n\t"
  "v_add_u32_e32 v226, s50, v224\n\t"
  "v_mfma_f32_16x16x32_bf16 v[252:255], v[232:235], a[192:195], 0\n\t"
  "ds_read_b128 a[32:35], v216 offset:49152\n\t"
  "ds_read_b128 a[36:39], v216 offset:49664\n\t"
  "v_mfma_f32_16x16x32_bf16 v[252:255], v[232:235], a[196:199], v[252:255]\n\t"
  "ds_read_b128 a[40:43], v216 offset:50176\n\t"
  "ds_read_b128 a[44:47], v216 offset:50688\n\t"
  "v_mfma_f32_16x16x32_bf16 v[252:255], v[232:235], a[200:203], v[252:255]\n\t"
  "s_mov_b64 exec, s[64:65]\n\t"
  "s_cbranch_execz LOW0%=\n\t"
  "ds_write_b128 v212, v[0:3] offset:0\n\t"
  "ds_write_b128 v212, v[8:11] offset:4096\n\t"
  "ds_write_b128 v212, v[16:19] offset:8192\n\t"
  "ds_write_b128 v212, v[24:27] offset:12288\n\t"
  "LOW0%=:\n\t"
  "s_mov_b64 exec, -1\n\t"
  "ds_read_b128 v[0:3], v226 offset:0\n\t"
  "ds_read_b128 v[4:7], v226 offset:1024\n\t"
  "ds_read_b128 v[8:11], v226 offset:2048\n\t"
  "ds_read_b128 v[12:15], v226 offset:3072\n\t"
  "ds_read_b128 v[16:19], v226 offset:4096\n\t"
  "ds_read_b128 v[20:23], v226 offset:5120\n\t"
  "ds_read_b128 v[24:27], v226 offset:6144\n\t"
  "ds_read_b128 v[28:31], v226 offset:7168\n\t"
  "ds_read_b128 a[48:51], v217 offset:49152\n\t"
  "ds_read_b128 a[52:55], v217 offset:49664\n\t"
  "v_mfma_f32_16x16x32_bf16 v[252:255], v[232:235], a[204:207], v[252:255]\n\t"
  "buffer_store_dwordx4 v[64:67], v222, %8, s63 offen offset:0 sc1\n\t"
  "ds_read_b128 a[56:59], v217 offset:50176\n\t"
  "ds_read_b128 a[60:63], v217 offset:50688\n\t"
  "v_mfma_f32_16x16x32_bf16 v[252:255], v[232:235], a[208:211], v[252:255]\n\t"
  "s_mov_b64 exec, s[66:67]\n\t"
  "s_cbranch_execz LOW1%=\n\t"
  "ds_write_b128 v212, v[32:35] offset:256\n\t"
  "ds_write_b128 v212, v[40:43] offset:4352\n\t"
  "ds_write_b128 v212, v[48:51] offset:8448\n\t"
  "ds_write_b128 v212, v[56:59] offset:12544\n\t"
  "LOW1%=:\n\t"
  "s_mov_b64 exec, -1\n\t"
  "ds_read_b128 v[32:35], v226 offset:8192\n\t"
  "ds_read_b128 v[36:39], v226 offset:9216\n\t"
  "ds_read_b128 v[40:43], v226 offset:10240\n\t"
  "ds_read_b128 v[44:47], v226 offset:11264\n\t"
  "ds_read_b128 v[48:51], v226 offset:12288\n\t"
  "ds_read_b128 v[52:55], v226 offset:13312\n\t"
  "ds_read_b128 v[56:59], v226 offset:14336\n\t"
  "ds_read_b128 v[60:63], v226 offset:15360\n\t"
  "ds_read_b64_tr_b16 a[96:97], v218 offset:49152\n\t"
  "ds_read_b64_tr_b16 a[98:99], v219 offset:49152\n\t"
  "v_mfma_f32_16x16x32_bf16 v[252:255], v[232:235], a[212:215], v[252:255]\n\t"
  "ds_read_b64_tr_b16 a[100:101], v196 offset:49152\n\t"
  "ds_read_b64_tr_b16 a[102:103], v197 offset:49152\n\t"
  "v_mfma_f32_16x16x32_bf16 v[252:255], v[232:235], a[216:219], v[252:255]\n\t"
  "ds_read_b64_tr_b16 a[104:105], v218 offset:49664\n\t"
  "ds_read_b64_tr_b16 a[106:107], v219 offset:49664\n\t"
  "v_mfma_f32_16x16x32_bf16 v[252:255], v[232:235], a[220:223], v[252:255]\n\t"
  "ds_read_b64_tr_b16 a[108:109], v196 offset:49664\n\t"
  "ds_read_b64_tr_b16 a[110:111], v197 offset:49664\n\t"
  "v_mfma_f32_16x16x32_bf16 v[252:255], v[236:239], a[224:227], v[252:255]\n\t"
  "buffer_store_dwordx4 v[72:75], v222, %8, s63 offen offset:1024 sc1\n\t"
  "ds_read_b64_tr_b16 a[112:113], v218 offset:50176\n\t"
  "ds_read_b64_tr_b16 a[114:115], v219 offset:50176\n\t"
  "v_mfma_f32_16x16x32_bf16 v[252:255], v[236:239], a[228:231], v[252:255]\n\t"
  "ds_read_b64_tr_b16 a[116:117], v196 offset:50176\n\t"
  "ds_read_b64_tr_b16 a[118:119], v197 offset:50176\n\t"
  "v_mfma_f32_16x16x32_bf16 v[252:255], v[236:239], a[232:235], v[252:255]\n\t"
  "ds_read_b64_tr_b16 a[120:121], v218 offset:50688\n\t"
  "ds_read_b64_tr_b16 a[122:123], v219 offset:50688\n\t"
  "v_mfma_f32_16x16x32_bf16 v[252:255], v[236:239], a[236:239], v[252:255]\n\t"
  "ds_read_b64_tr_b16 a[124:125], v196 offset:50688\n\t"
  "ds_read_b64_tr_b16 a[126:127], v197 offset:50688\n\t"
  "s_waitcnt lgkmcnt(15)\n\t"
  "v_mfma_f32_16x16x32_bf16 v[160:163], a[32:35], a[16:19], 0\n\t"
  "buffer_store_dwordx4 v[80:83], v222, %8, s63 offen offset:2048 sc1\n\t"
  "v_or3_b32 v198, v232, v234, v236\n\t"
  "v_or3_b32 v199, v233, v235, v237\n\t"
  "v_lshrrev_b32_e32 v196, 2, v227\n\t"
  "v_or_b32_e32 v198, v198, v238\n\t"
  "v_mfma_f32_16x16x32_bf16 v[164:167], a[48:51], a[16:19], 0\n\t"
  "v_or_b32_e32 v199, v199, v239\n\t"
  "v_and_b32_e32 v196, 48, v196\n\t"
  "v_cmp_eq_u32_e32 vcc, v196, v207\n\t"
  "v_mov_b32_e32 v196, 0\n\t"
  "v_mfma_f32_16x16x32_bf16 v[160:163], a[36:39], a[20:23], v[160:163]\n\t"
  "v_mov_b32_e32 v197, 0\n\t"
  "v_mov_b32_e32 v200, 0\n\t"
  "v_mov_b32_e32 v201, 0\n\t"
  "v_cndmask_b32_e32 v198, 0, v198, vcc\n\t"
  "v_mfma_f32_16x16x32_bf16 v[164:167], a[52:55], a[20:23], v[164:167]\n\t"
  "v_cndmask_b32_e32 v199, 0, v199, vcc\n\t"
  "v_readlane_b32 s78, v214, 4\n\t"
  "v_readlane_b32 s79, v214, 5\n\t"
  "v_readlane_b32 s80, v214, 6\n\t"
  "v_mfma_f32_16x16x32_bf16 v[160:163], a[40:43], a[24:27], v[160:163]\n\t"
  "v_readlane_b32 s81, v214, 7\n\t"
  "v_readlane_b32 s82, v214, 8\n\t"
  "v_readlane_b32 s83, v214, 9\n\t"
  "v_readlane_b32 s84, v214, 10\n\t"
  "v_mfma_f32_16x16x32_bf16 v[164:167], a[56:59], a[24:27], v[164:167]\n\t"
  "buffer_store_dwordx4 v[88:91], v222, %8, s63 offen offset:3072 sc1\n\t"
  "v_readlane_b32 s85, v214, 11\n\t"
  "v_readlane_b32 s86, v214, 12\n\t"
  "v_mov_b32_e32 v202, s78\n\t"
  "v_mov_b32_e32 v203, s80\n\t"
  "v_mfma_f32_16x16x32_bf16 v[160:163], a[44:47], a[28:31], v[160:163]\n\t"
  "s_add_u32 s63, s63, 0x1000\n\t"
  "s_mov_b64 exec, s[68:69]\n\t"
  "s_cbranch_execz LOW2%=\n\t"
  "ds_write_b128 v212, v[64:67] offset:512\n\t"
  "ds_write_b128 v212, v[72:75] offset:4608\n\t"
  "ds_write_b128 v212, v[80:83] offset:8704\n\t"
  "ds_write_b128 v212, v[88:91] offset:12800\n\t"
  "LOW2%=:\n\t"
  "s_mov_b64 exec, -1\n\t"
  "ds_read_b128 v[64:67], v226 offset:16384\n\t"
  "ds_read_b128 v[68:71], v226 offset:17408\n\t"
  "ds_read_b128 v[72:75], v226 offset:18432\n\t"
  "ds_read_b128 v[76:79], v226 offset:19456\n\t"
  "ds_read_b128 v[80:83], v226 offset:20480\n\t"
  "ds_read_b128 v[84:87], v226 offset:21504\n\t"
  "ds_read_b128 v[88:91], v226 offset:22528\n\t"
  "ds_read_b128 v[92:95], v226 offset:23552\n\t"
  "v_mov_b32_e32 v204, s82\n\t"
  "v_mov_b32_e32 v205, s84\n\t"
  "v_mov_b32_e32 v206, s86\n\t"
  "v_mfma_f32_16x16x32_bf16 v[164:167], a[60:63], a[28:31], v[164:167]\n\t"
  "s_mov_b32 exec_lo, 0\n\t"
  "v_mov_b32_e32 v202, s79\n\t"
  "v_mov_b32_e32 v203, s81\n\t"
  "v_mov_b32_e32 v204, s83\n\t"
  "v_mov_b32_e32 v205, s85\n\t"
  "s_mov_b32 exec_lo, -1\n\t"
  "v_mfma_f32_16x16x32_bf16 v[252:255], v[236:239], a[240:243], v[252:255]\n\t"
  "v_mfma_f32_16x16x32_bf16 v[252:255], v[236:239], a[244:247], v[252:255]\n\t"
  "v_exp_f32_e32 v160, v160\n\t"
  "v_mfma_f32_16x16x32_bf16 v[252:255], v[236:239], a[248:251], v[252:255]\n\t"
  "buffer_store_dwordx4 v[96:99], v223, %8, s63 offen offset:0 sc1\n\t"
  "v_exp_f32_e32 v161, v161\n\t"
  "v_mfma_f32_16x16x32_bf16 v[252:255], v[236:239], a[252:255], v[252:255]\n\t"
  "v_exp_f32_e32 v162, v162\n\t"
  "v_mfma_f32_16x16x32_bf16 v[192:195], v[232:235], a[128:131], 0\n\t"
  "v_exp_f32_e32 v163, v163\n\t"
  "v_mfma_f32_16x16x32_bf16 v[192:195], v[232:235], a[132:135], v[192:195]\n\t"
  "v_exp_f32_e32 v164, v164\n\t"
  "v_mfma_f32_16x16x32_bf16 v[192:195], v[232:235], a[136:139], v[192:195]\n\t"
  "v_exp_f32_e32 v165, v165\n\t"
  "v_mfma_f32_16x16x32_bf16 v[192:195], v[232:235], a[140:143], v[192:195]\n\t"
  "buffer_store_dwordx4 v[104:107], v223, %8, s63 offen offset:1024 sc1\n\t"
  "v_exp_f32_e32 v166, v166\n\t"
  "v_mfma_f32_16x16x32_bf16 v[192:195], v[232:235], a[144:147], v[192:195]\n\t"
  "v_exp_f32_e32 v167, v167\n\t"
  "v_mfma_f32_16x16x32_bf16 v[192:195], v[232:235], a[148:151], v[192:195]\n\t"
  "v_cvt_pk_bf16_f32 v160, v160, v161\n\t"
  "v_mfma_f32_16x16x32_bf16 v[192:195], v[232:235], a[152:155], v[192:195]\n\t"
  "v_cvt_pk_bf16_f32 v161, v162, v163\n\t"
  "v_mfma_f32_16x16x32_bf16 v[192:195], v[232:235], a[156:159], v[192:195]\n\t"
  "buffer_store_dwordx4 v[112:115], v223, %8, s63 offen offset:2048 sc1\n\t"
  "v_cvt_pk_bf16_f32 v162, v164, v165\n\t"
  "v_cvt_pk_bf16_f32 v163, v166, v167\n\t"
  "s_waitcnt lgkmcnt(0)\n\t"
  "s_nop 0\n\t"
  "v_mfma_f32_16x16x32_bf16 v[128:131], a[96:99], v[160:163], 0\n\t"
  "v_and_b32_e32 v202, 0xffff0000, v202\n\t"
  "v_and_b32_e32 v203, 0xffff0000, v203\n\t"
  "v_and_b32_e32 v204, 0xffff0000, v204\n\t"
  "v_and_b32_e32 v205, 0xffff0000, v205\n\t"
  "v_mfma_f32_16x16x32_bf16 v[132:135], a[100:103], v[160:163], 0\n\t"
  "v_and_b32_e32 v206, 0xffff0000, v206\n\t"
  "s_and_b32 s78, s78, 0x7f\n\t"
  "s_and_b32 s79, s79, 0x7f\n\t"
  "s_and_b32 s80, s80, 0x7f\n\t"
  "v_mfma_f32_16x16x32_bf16 v[136:139], a[104:107], v[160:163], 0\n\t"
  "s_and_b32 s81, s81, 0x7f\n\t"
  "s_and_b32 s82, s82, 0x7f\n\t"
  "s_and_b32 s83, s83, 0x7f\n\t"
  "s_and_b32 s84, s84, 0x7f\n\t"
  "v_mfma_f32_16x16x32_bf16 v[140:143], a[108:111], v[160:163], 0\n\t"
  "s_and_b32 s85, s85, 0x7f\n\t"
  "s_and_b32 s86, s86, 0x7f\n\t"
  "s_bitcmp1_b32 s87, 4\n\t"
  "s_cselect_b32 s79, 48, s79\n\t"
  "v_mfma_f32_16x16x32_bf16 v[144:147], a[112:115], v[160:163], 0\n\t"
  "buffer_store_dwordx4 v[120:123], v223, %8, s63 offen offset:3072 sc1\n\t"
  "s_bitcmp1_b32 s87, 6\n\t"
  "s_cselect_b32 s81, 48, s81\n\t"
  "s_bitcmp1_b32 s87, 8\n\t"
  "s_cselect_b32 s83, 48, s83\n\t"
  "v_mfma_f32_16x16x32_bf16 v[148:151], a[116:119], v[160:163], 0\n\t"
  "s_bitcmp1_b32 s87, 10\n\t"
  "s_cselect_b32 s85, 48, s85\n\t"
  "v_lshl_add_u32 v225, v207, 6, v227\n\t"
  "v_add_u32_e32 v225, s41, v225\n\t"
  "v_mfma_f32_16x16x32_bf16 v[152:155], a[120:123], v[160:163], 0\n\t"
  "s_mov_b32 s73, 0\n\t"
  "v_mfma_f32_16x16x32_bf16 v[156:159], a[124:127], v[160:163], 0\n\t"
  "ds_read_b128 v[160:163], v229 offset:16\n\t"
  "ds_read_b128 v[164:167], v229 offset:144\n\t"
  "ds_read_b128 v[168:171], v229 offset:272\n\t"
  "ds_read_b128 v[172:175], v229 offset:400\n\t"
  "ds_read_b128 v[176:179], v229 offset:528\n\t"
  "ds_read_b128 v[180:183], v229 offset:656\n\t"
  "ds_read_b128 v[184:187], v229 offset:784\n\t"
  "ds_read_b128 v[188:191], v229 offset:912\n\t"
  "ds_read_b128 v[208:211], v229 offset:1040\n\t"
  "s_mov_b64 exec, s[70:71]\n\t"
  "s_cbranch_execz LOW3%=\n\t"
  "ds_write_b128 v212, v[96:99] offset:768\n\t"
  "ds_write_b128 v212, v[104:107] offset:4864\n\t"
  "ds_write_b128 v212, v[112:115] offset:8960\n\t"
  "ds_write_b128 v212, v[120:123] offset:13056\n\t"
  "LOW3%=:\n\t"
  "s_mov_b64 exec, -1\n\t"
  "s_waitcnt lgkmcnt(0)\n\t"
  "s_cmp_gt_u32 s78, 32\n\t"
  "s_cbranch_scc0 LI0%=\n\t"
  "s_add_u32 s73, s73, 1\n\t"
  "v_mad_u32_u16 v217, v160, s90, v227\n\t"
  "buffer_load_dwordx4 a[32:35], v217, %7, 0 offen offset:1024\n\t"
  "s_cmp_gt_u32 s78, 36\n\t"
  "s_cbranch_scc0 LI0%=\n\t"
  "s_add_u32 s73, s73, 1\n\t"
  "v_mad_u32_u16 v218, v160, s90, v227 op_sel:[1,0,0,0]\n\t"
  "buffer_load_dwordx4 a[36:39], v218, %7, 0 offen offset:1024\n\t"
  "s_cmp_gt_u32 s78, 40\n\t"
  "s_cbranch_scc0 LI0%=\n\t"
  "s_add_u32 s73, s73, 1\n\t"
  "v_mad_u32_u16 v217, v161, s90, v227\n\t"
  "buffer_load_dwordx4 a[40:43], v217, %7, 0 offen offset:1024\n\t"
  "s_cmp_gt_u32 s78, 44\n\t"
  "s_cbranch_scc0 LI0%=\n\t"
  "s_add_u32 s73, s73, 1\n\t"
  "v_mad_u32_u16 v218, v161, s90, v227 op_sel:[1,0,0,0]\n\t"
  "buffer_load_dwordx4 a[44:47], v218, %7, 0 offen offset:1024\n\t"
  "s_cmp_gt_u32 s78, 48\n\t"
  "s_cbranch_scc0 LI0%=\n\t"
  "s_add_u32 s73, s73, 1\n\t"
  "v_mad_u32_u16 v217, v162, s90, v227\n\t"
  "buffer_load_dwordx4 a[176:179], v217, %7, 0 offen offset:1024\n\t"
  "v_mad_u32_u16 v218, v162, s90, v227 op_sel:[1,0,0,0]\n\t"
  "buffer_load_dwordx4 a[180:183], v218, %7, 0 offen offset:1024\n\t"
  "v_mad_u32_u16 v217, v163, s90, v227\n\t"
  "buffer_load_dwordx4 a[184:187], v217, %7, 0 offen offset:1024\n\t"
  "v_mad_u32_u16 v218, v163, s90, v227 op_sel:[1,0,0,0]\n\t"
  "buffer_load_dwordx4 a[188:191], v218, %7, 0 offen offset:1024\n\t"
  "LI0%=:\n\t"
  "s_cmp_gt_u32 s79, 32\n\t"
  "s_cbranch_scc0 LI1%=\n\t"
  "s_add_u32 s73, s73, 1\n\t"
  "v_mad_u32_u16 v217, v164, s90, v227\n\t"
  "buffer_load_dwordx4 a[48:51], v217, %7, 0 offen offset:1024\n\t"
  "s_cmp_gt_u32 s79, 36\n\t"
  "s_cbranch_scc0 LI1%=\n\t"
  "s_add_u32 s73, s73, 1\n\t"
  "v_mad_u32_u16 v218, v164, s90, v227 op_sel:[1,0,0,0]\n\t"
  "buffer_load_dwordx4 a[52:55], v218, %7, 0 offen offset:1024\n\t"
  "s_cmp_gt_u32 s79, 40\n\t"
  "s_cbranch_scc0 LI1%=\n\t"
  "s_add_u32 s73, s73, 1\n\t"
  "v_mad_u32_u16 v217, v165, s90, v227\n\t"
  "buffer_load_dwordx4 a[56:59], v217, %7, 0 offen offset:1024\n\t"
  "s_cmp_gt_u32 s79, 44\n\t"
  "s_cbranch_scc0 LI1%=\n\t"
  "s_add_u32 s73, s73, 1\n\t"
  "v_mad_u32_u16 v218, v165, s90, v227 op_sel:[1,0,0,0]\n\t"
  "buffer_load_dwordx4 a[60:63], v218, %7, 0 offen offset:1024\n\t"
  "s_cmp_gt_u32 s79, 48\n\t"
  "s_cbranch_scc0 LI1%=\n\t"
  "s_add_u32 s73, s73, 1\n\t"
  "v_mad_u32_u16 v217, v166, s90, v227\n\t"
  "buffer_load_dwordx4 a[176:179], v217, %7, 0 offen offset:1024\n\t"
  "v_mad_u32_u16 v218, v166, s90, v227 op_sel:[1,0,0,0]\n\t"
  "buffer_load_dwordx4 a[180:183], v218, %7, 0 offen offset:1024\n\t"
  "v_mad_u32_u16 v217, v167, s90, v227\n\t"
  "buffer_load_dwordx4 a[184:187], v217, %7, 0 offen offset:1024\n\t"
  "v_mad_u32_u16 v218, v167, s90, v227 op_sel:[1,0,0,0]\n\t"
  "buffer_load_dwordx4 a[188:191], v218, %7, 0 offen offset:1024\n\t"
  "LI1%=:\n\t"
  "s_cmp_gt_u32 s80, 32\n\t"
  "s_cbranch_scc0 LI2%=\n\t"
  "s_add_u32 s73, s73, 1\n\t"
  "v_mad_u32_u16 v217, v168, s90, v227\n\t"
  "buffer_load_dwordx4 a[64:67], v217, %7, 0 offen offset:1024\n\t"
  "s_cmp_gt_u32 s80, 36\n\t"
  "s_cbranch_scc0 LI2%=\n\t"
  "s_add_u32 s73, s73, 1\n\t"
  "v_mad_u32_u16 v218, v168, s90, v227 op_sel:[1,0,0,0]\n\t"
  "buffer_load_dwordx4 a[68:71], v218, %7, 0 offen offset:1024\n\t"
  "s_cmp_gt_u32 s80, 40\n\t"
  "s_cbranch_scc0 LI2%=\n\t"
  "s_add_u32 s73, s73, 1\n\t"
  "v_mad_u32_u16 v217, v169, s90, v227\n\t"
  "buffer_load_dwordx4 a[72:75], v217, %7, 0 offen offset:1024\n\t"
  "s_cmp_gt_u32 s80, 44\n\t"
  "s_cbranch_scc0 LI2%=\n\t"
  "s_add_u32 s73, s73, 1\n\t"
  "v_mad_u32_u16 v218, v169, s90, v227 op_sel:[1,0,0,0]\n\t"
  "buffer_load_dwordx4 a[76:79], v218, %7, 0 offen offset:1024\n\t"
  "s_cmp_gt_u32 s80, 48\n\t"
  "s_cbranch_scc0 LI2%=\n\t"
  "s_add_u32 s73, s73, 1\n\t"
  "v_mad_u32_u16 v217, v170, s90, v227\n\t"
  "buffer_load_dwordx4 a[192:195], v217, %7, 0 offen offset:1024\n\t"
  "v_mad_u32_u16 v218, v170, s90, v227 op_sel:[1,0,0,0]\n\t"
  "buffer_load_dwordx4 a[196:199], v218, %7, 0 offen offset:1024\n\t"
  "v_mad_u32_u16 v217, v171, s90, v227\n\t"
  "buffer_load_dwordx4 a[200:203], v217, %7, 0 offen offset:1024\n\t"
  "v_mad_u32_u16 v218, v171, s90, v227 op_sel:[1,0,0,0]\n\t"
  "buffer_load_dwordx4 a[204:207], v218, %7, 0 offen offset:1024\n\t"
  "LI2%=:\n\t"
  "s_cmp_gt_u32 s81, 32\n\t"
  "s_cbranch_scc0 LI3%=\n\t"
  "s_add_u32 s73, s73, 1\n\t"
  "v_mad_u32_u16 v217, v172, s90, v227\n\t"
  "buffer_load_dwordx4 a[80:83], v217, %7, 0 offen offset:1024\n\t"
  "s_cmp_gt_u32 s81, 36\n\t"
  "s_cbranch_scc0 LI3%=\n\t"
  "s_add_u32 s73, s73, 1\n\t"
  "v_mad_u32_u16 v218, v172, s90, v227 op_sel:[1,0,0,0]\n\t"
  "buffer_load_dwordx4 a[84:87], v218, %7, 0 offen offset:1024\n\t"
  "s_cmp_gt_u32 s81, 40\n\t"
  "s_cbranch_scc0 LI3%=\n\t"
  "s_add_u32 s73, s73, 1\n\t"
  "v_mad_u32_u16 v217, v173, s90, v227\n\t"
  "buffer_load_dwordx4 a[88:91], v217, %7, 0 offen offset:1024\n\t"
  "s_cmp_gt_u32 s81, 44\n\t"
  "s_cbranch_scc0 LI3%=\n\t"
  "s_add_u32 s73, s73, 1\n\t"
  "v_mad_u32_u16 v218, v173, s90, v227 op_sel:[1,0,0,0]\n\t"
  "buffer_load_dwordx4 a[92:95], v218, %7, 0 offen offset:1024\n\t"
  "s_cmp_gt_u32 s81, 48\n\t"
  "s_cbranch_scc0 LI3%=\n\t"
  "s_add_u32 s73, s73, 1\n\t"
  "v_mad_u32_u16 v217, v174, s90, v227\n\t"
  "buffer_load_dwordx4 a[192:195], v217, %7, 0 offen offset:1024\n\t"
  "v_mad_u32_u16 v218, v174, s90, v227 op_sel:[1,0,0,0]\n\t"
  "buffer_load_dwordx4 a[196:199], v218, %7, 0 offen offset:1024\n\t"
  "v_mad_u32_u16 v217, v175, s90, v227\n\t"
  "buffer_load_dwordx4 a[200:203], v217, %7, 0 offen offset:1024\n\t"
  "v_mad_u32_u16 v218, v175, s90, v227 op_sel:[1,0,0,0]\n\t"
  "buffer_load_dwordx4 a[204:207], v218, %7, 0 offen offset:1024\n\t"
  "LI3%=:\n\t"
  "s_cmp_gt_u32 s82, 32\n\t"
  "s_cbranch_scc0 LI4%=\n\t"
  "s_add_u32 s73, s73, 1\n\t"
  "v_mad_u32_u16 v217, v176, s90, v227\n\t"
  "buffer_load_dwordx4 a[96:99], v217, %7, 0 offen offset:1024\n\t"
  "s_cmp_gt_u32 s82, 36\n\t"
  "s_cbranch_scc0 LI4%=\n\t"
  "s_add_u32 s73, s73, 1\n\t"
  "v_mad_u32_u16 v218, v176, s90, v227 op_sel:[1,0,0,0]\n\t"
  "buffer_load_dwordx4 a[100:103], v218, %7, 0 offen offset:1024\n\t"
  "s_cmp_gt_u32 s82, 40\n\t"
  "s_cbranch_scc0 LI4%=\n\t"
  "s_add_u32 s73, s73, 1\n\t"
  "v_mad_u32_u16 v217, v177, s90, v227\n\t"
  "buffer_load_dwordx4 a[104:107], v217, %7, 0 offen offset:1024\n\t"
  "s_cmp_gt_u32 s82, 44\n\t"
  "s_cbranch_scc0 LI4%=\n\t"
  "s_add_u32 s73, s73, 1\n\t"
  "v_mad_u32_u16 v218, v177, s90, v227 op_sel:[1,0,0,0]\n\t"
  "buffer_load_dwordx4 a[108:111], v218, %7, 0 offen offset:1024\n\t"
  "s_cmp_gt_u32 s82, 48\n\t"
  "s_cbranch_scc0 LI4%=\n\t"
  "s_add_u32 s73, s73, 1\n\t"
  "v_mad_u32_u16 v217, v178, s90, v227\n\t"
  "buffer_load_dwordx4 a[208:211], v217, %7, 0 offen offset:1024\n\t"
  "v_mad_u32_u16 v218, v178, s90, v227 op_sel:[1,0,0,0]\n\t"
  "buffer_load_dwordx4 a[212:215], v218, %7, 0 offen offset:1024\n\t"
  "v_mad_u32_u16 v217, v179, s90, v227\n\t"
  "buffer_load_dwordx4 a[216:219], v217, %7, 0 offen offset:1024\n\t"
  "v_mad_u32_u16 v218, v179, s90, v227 op_sel:[1,0,0,0]\n\t"
  "buffer_load_dwordx4 a[220:223], v218, %7, 0 offen offset:1024\n\t"
  "LI4%=:\n\t"
  "s_cmp_gt_u32 s83, 32\n\t"
  "s_cbranch_scc0 LI5%=\n\t"
  "s_add_u32 s73, s73, 1\n\t"
  "v_mad_u32_u16 v217, v180, s90, v227\n\t"
  "buffer_load_dwordx4 a[112:115], v217, %7, 0 offen offset:1024\n\t"
  "s_cmp_gt_u32 s83, 36\n\t"
  "s_cbranch_scc0 LI5%=\n\t"
  "s_add_u32 s73, s73, 1\n\t"
  "v_mad_u32_u16 v218, v180, s90, v227 op_sel:[1,0,0,0]\n\t"
  "buffer_load_dwordx4 a[116:119], v218, %7, 0 offen offset:1024\n\t"
  "s_cmp_gt_u32 s83, 40\n\t"
  "s_cbranch_scc0 LI5%=\n\t"
  "s_add_u32 s73, s73, 1\n\t"
  "v_mad_u32_u16 v217, v181, s90, v227\n\t"
  "buffer_load_dwordx4 a[120:123], v217, %7, 0 offen offset:1024\n\t"
  "s_cmp_gt_u32 s83, 44\n\t"
  "s_cbranch_scc0 LI5%=\n\t"
  "s_add_u32 s73, s73, 1\n\t"
  "v_mad_u32_u16 v218, v181, s90, v227 op_sel:[1,0,0,0]\n\t"
  "buffer_load_dwordx4 a[124:127], v218, %7, 0 offen offset:1024\n\t"
  "s_cmp_gt_u32 s83, 48\n\t"
  "s_cbranch_scc0 LI5%=\n\t"
  "s_add_u32 s73, s73, 1\n\t"
  "v_mad_u32_u16 v217, v182, s90, v227\n\t"
  "buffer_load_dwordx4 a[208:211], v217, %7, 0 offen offset:1024\n\t"
  "v_mad_u32_u16 v218, v182, s90, v227 op_sel:[1,0,0,0]\n\t"
  "buffer_load_dwordx4 a[212:215], v218, %7, 0 offen offset:1024\n\t"
  "v_mad_u32_u16 v217, v183, s90, v227\n\t"
  "buffer_load_dwordx4 a[216:219], v217, %7, 0 offen offset:1024\n\t"
  "v_mad_u32_u16 v218, v183, s90, v227 op_sel:[1,0,0,0]\n\t"
  "buffer_load_dwordx4 a[220:223], v218, %7, 0 offen offset:1024\n\t"
  "LI5%=:\n\t"
  "s_cmp_gt_u32 s84, 32\n\t"
  "s_cbranch_scc0 LI6%=\n\t"
  "s_add_u32 s73, s73, 1\n\t"
  "v_mad_u32_u16 v217, v184, s90, v227\n\t"
  "buffer_load_dwordx4 a[128:131], v217, %7, 0 offen offset:1024\n\t"
  "s_cmp_gt_u32 s84, 36\n\t"
  "s_cbranch_scc0 LI6%=\n\t"
  "s_add_u32 s73, s73, 1\n\t"
  "v_mad_u32_u16 v218, v184, s90, v227 op_sel:[1,0,0,0]\n\t"
  "buffer_load_dwordx4 a[132:135], v218, %7, 0 offen offset:1024\n\t"
  "s_cmp_gt_u32 s84, 40\n\t"
  "s_cbranch_scc0 LI6%=\n\t"
  "s_add_u32 s73, s73, 1\n\t"
  "v_mad_u32_u16 v217, v185, s90, v227\n\t"
  "buffer_load_dwordx4 a[136:139], v217, %7, 0 offen offset:1024\n\t"
  "s_cmp_gt_u32 s84, 44\n\t"
  "s_cbranch_scc0 LI6%=\n\t"
  "s_add_u32 s73, s73, 1\n\t"
  "v_mad_u32_u16 v218, v185, s90, v227 op_sel:[1,0,0,0]\n\t"
  "buffer_load_dwordx4 a[140:143], v218, %7, 0 offen offset:1024\n\t"
  "s_cmp_gt_u32 s84, 48\n\t"
  "s_cbranch_scc0 LI6%=\n\t"
  "s_add_u32 s73, s73, 1\n\t"
  "v_mad_u32_u16 v217, v186, s90, v227\n\t"
  "buffer_load_dwordx4 a[224:227], v217, %7, 0 offen offset:1024\n\t"
  "v_mad_u32_u16 v218, v186, s90, v227 op_sel:[1,0,0,0]\n\t"
  "buffer_load_dwordx4 a[228:231], v218, %7, 0 offen offset:1024\n\t"
  "v_mad_u32_u16 v217, v187, s90, v227\n\t"
  "buffer_load_dwordx4 a[232:235], v217, %7, 0 offen offset:1024\n\t"
  "v_mad_u32_u16 v218, v187, s90, v227 op_sel:[1,0,0,0]\n\t"
  "buffer_load_dwordx4 a[236:239], v218, %7, 0 offen offset:1024\n\t"
  "LI6%=:\n\t"
  "s_cmp_gt_u32 s85, 32\n\t"
  "s_cbranch_scc0 LI7%=\n\t"
  "s_add_u32 s73, s73, 1\n\t"
  "v_mad_u32_u16 v217, v188, s90, v227\n\t"
  "buffer_load_dwordx4 a[144:147], v217, %7, 0 offen offset:1024\n\t"
  "s_cmp_gt_u32 s85, 36\n\t"
  "s_cbranch_scc0 LI7%=\n\t"
  "s_add_u32 s73, s73, 1\n\t"
  "v_mad_u32_u16 v218, v188, s90, v227 op_sel:[1,0,0,0]\n\t"
  "buffer_load_dwordx4 a[148:151], v218, %7, 0 offen offset:1024\n\t"
  "s_cmp_gt_u32 s85, 40\n\t"
  "s_cbranch_scc0 LI7%=\n\t"
  "s_add_u32 s73, s73, 1\n\t"
  "v_mad_u32_u16 v217, v189, s90, v227\n\t"
  "buffer_load_dwordx4 a[152:155], v217, %7, 0 offen offset:1024\n\t"
  "s_cmp_gt_u32 s85, 44\n\t"
  "s_cbranch_scc0 LI7%=\n\t"
  "s_add_u32 s73, s73, 1\n\t"
  "v_mad_u32_u16 v218, v189, s90, v227 op_sel:[1,0,0,0]\n\t"
  "buffer_load_dwordx4 a[156:159], v218, %7, 0 offen offset:1024\n\t"
  "s_cmp_gt_u32 s85, 48\n\t"
  "s_cbranch_scc0 LI7%=\n\t"
  "s_add_u32 s73, s73, 1\n\t"
  "v_mad_u32_u16 v217, v190, s90, v227\n\t"
  "buffer_load_dwordx4 a[224:227], v217, %7, 0 offen offset:1024\n\t"
  "v_mad_u32_u16 v218, v190, s90, v227 op_sel:[1,0,0,0]\n\t"
  "buffer_load_dwordx4 a[228:231], v218, %7, 0 offen offset:1024\n\t"
  "v_mad_u32_u16 v217, v191, s90, v227\n\t"
  "buffer_load_dwordx4 a[232:235], v217, %7, 0 offen offset:1024\n\t"
  "v_mad_u32_u16 v218, v191, s90, v227 op_sel:[1,0,0,0]\n\t"
  "buffer_load_dwordx4 a[236:239], v218, %7, 0 offen offset:1024\n\t"
  "LI7%=:\n\t"
  "s_cmp_gt_u32 s86, 32\n\t"
  "s_cbranch_scc0 LI8%=\n\t"
  "s_add_u32 s73, s73, 1\n\t"
  "v_mad_u32_u16 v217, v208, s90, v227\n\t"
  "buffer_load_dwordx4 a[160:163], v217, %7, 0 offen offset:1024\n\t"
  "s_cmp_gt_u32 s86, 36\n\t"
  "s_cbranch_scc0 LI8%=\n\t"
  "s_add_u32 s73, s73, 1\n\t"
  "v_mad_u32_u16 v218, v208, s90, v227 op_sel:[1,0,0,0]\n\t"
  "buffer_load_dwordx4 a[164:167], v218, %7, 0 offen offset:1024\n\t"
  "s_cmp_gt_u32 s86, 40\n\t"
  "s_cbranch_scc0 LI8%=\n\t"
  "s_add_u32 s73, s73, 1\n\t"
  "v_mad_u32_u16 v217, v209, s90, v227\n\t"
  "buffer_load_dwordx4 a[168:171], v217, %7, 0 offen offset:1024\n\t"
  "s_cmp_gt_u32 s86, 44\n\t"
  "s_cbranch_scc0 LI8%=\n\t"
  "s_add_u32 s73, s73, 1\n\t"
  "v_mad_u32_u16 v218, v209, s90, v227 op_sel:[1,0,0,0]\n\t"
  "buffer_load_dwordx4 a[172:175], v218, %7, 0 offen offset:1024\n\t"
  "s_cmp_gt_u32 s86, 48\n\t"
  "s_cbranch_scc0 LI8%=\n\t"
  "s_add_u32 s73, s73, 1\n\t"
  "v_mad_u32_u16 v217, v210, s90, v227\n\t"
  "buffer_load_dwordx4 a[240:243], v217, %7, 0 offen offset:1024\n\t"
  "v_mad_u32_u16 v218, v210, s90, v227 op_sel:[1,0,0,0]\n\t"
  "buffer_load_dwordx4 a[244:247], v218, %7, 0 offen offset:1024\n\t"
  "v_mad_u32_u16 v217, v211, s90, v227\n\t"
  "buffer_load_dwordx4 a[248:251], v217, %7, 0 offen offset:1024\n\t"
  "v_mad_u32_u16 v218, v211, s90, v227 op_sel:[1,0,0,0]\n\t"
  "buffer_load_dwordx4 a[252:255], v218, %7, 0 offen offset:1024\n\t"
  "LI8%=:\n\t"
  "v_mfma_f32_16x16x32_bf16 v[160:163], v[198:201], a[0:3], 0\n\t"
  "ds_read_b128 v[96:99], v226 offset:24576\n\t"
  "ds_read_b128 v[100:103], v226 offset:25600\n\t"
  "v_mfma_f32_16x16x32_bf16 v[164:167], v[196:199], a[0:3], 0\n\t"
  "ds_read_b128 v[104:107], v226 offset:26624\n\t"
  "ds_read_b128 v[108:111], v226 offset:27648\n\t"
  "v_mfma_f32_16x16x32_bf16 v[168:171], v[198:201], a[4:7], 0\n\t"
  "ds_read_b128 v[112:115], v226 offset:28672\n\t"
  "ds_read_b128 v[116:119], v226 offset:29696\n\t"
  "v_mfma_f32_16x16x32_bf16 v[172:175], v[196:199], a[4:7], 0\n\t"
  "ds_read_b128 v[120:123], v226 offset:30720\n\t"
  "ds_read_b128 v[124:127], v226 offset:31744\n\t"
  "v_mfma_f32_16x16x32_bf16 v[176:179], v[198:201], a[8:11], 0\n\t"
  "v_add3_u32 v220, v224, v207, s42\n\t"
  "v_mad_u32_u24 v221, v227, 17, v207\n\t"
  "v_mfma_f32_16x16x32_bf16 v[180:183], v[196:199], a[8:11], 0\n\t"
  "v_add_u32_e32 v221, s42, v221\n\t"
  "v_lshrrev_b32_e32 v222, 5, v207\n\t"
  "v_mfma_f32_16x16x32_bf16 v[184:187], v[198:201], a[12:15], 0\n\t"
  "v_mul_u32_u24_e32 v222, 0x110, v222\n\t"
  "v_bfe_u32 v223, v207, 4, 1\n\t"
  "v_mfma_f32_16x16x32_bf16 v[188:191], v[196:199], a[12:15], 0\n\t"
  "v_lshl_add_u32 v222, v223, 3, v222\n\t"
  "v_add3_u32 v212, v222, v227, s42\n\t"
  "v_mov_b32_e32 v217, 1\n\t"
  "v_mov_b32_e32 v218, s49\n\t"
  "s_waitcnt vmcnt(13)\n\t"
  "s_cmp_ge_u32 s73, 13\n\t"
  "s_cbranch_scc1 LPUB%=\n\t"
  "s_waitcnt vmcnt(10)\n\t"
  "s_cmp_ge_u32 s73, 10\n\t"
  "s_cbranch_scc1 LPUB%=\n\t"
  "s_waitcnt vmcnt(8)\n\t"
  "s_cmp_ge_u32 s73, 8\n\t"
  "s_cbranch_scc1 LPUB%=\n\t"
  "s_waitcnt vmcnt(6)\n\t"
  "s_cmp_ge_u32 s73, 6\n\t"
  "s_cbranch_scc1 LPUB%=\n\t"
  "s_waitcnt vmcnt(4)\n\t"
  "s_cmp_ge_u32 s73, 4\n\t"
  "s_cbranch_scc1 LPUB%=\n\t"
  "s_waitcnt vmcnt(0)\n\t"
  "LPUB%=:\n\t"
  "s_waitcnt lgkmcnt(0)\n\t"
  "s_barrier\n\t"
  "s_mov_b64 exec, 1\n\t"
  "buffer_store_dword v217, v218, %8, 0 offen sc1\n\t"
  "s_mov_b64 exec, -1\n\t"
  "ds_read_b128 a[16:19], v225 offset:0\n\t"
  "ds_read_b128 a[20:23], v225 offset:4096\n\t"
  "ds_read_b128 a[24:27], v225 offset:8192\n\t"
  "ds_read_b128 a[28:31], v225 offset:12288\n\t"
  "s_waitcnt vmcnt(1)\n\t"
  "s_cmp_gt_u32 s78, 32\n\t"
  "s_cbranch_scc0 LS0%=\n\t"
  "v_mfma_f32_16x16x32_bf16 v[240:243], v[232:235], a[32:35], v[240:243]\n\t"
  "s_cmp_gt_u32 s78, 36\n\t"
  "s_cbranch_scc0 LS0%=\n\t"
  "v_mfma_f32_16x16x32_bf16 v[240:243], v[232:235], a[36:39], v[240:243]\n\t"
  "s_cmp_gt_u32 s78, 40\n\t"
  "s_cbranch_scc0 LS0%=\n\t"
  "v_mfma_f32_16x16x32_bf16 v[240:243], v[232:235], a[40:43], v[240:243]\n\t"
  "s_cmp_gt_u32 s78, 44\n\t"
  "s_cbranch_scc0 LS0%=\n\t"
  "v_mfma_f32_16x16x32_bf16 v[240:243], v[232:235], a[44:47], v[240:243]\n\t"
  "s_cmp_gt_u32 s78, 48\n\t"
  "s_cbranch_scc0 LS0%=\n\t"
  "v_mfma_f32_16x16x32_bf16 v[240:243], v[232:235], a[176:179], v[240:243]\n\t"
  "v_mfma_f32_16x16x32_bf16 v[240:243], v[232:235], a[180:183], v[240:243]\n\t"
  "v_mfma_f32_16x16x32_bf16 v[240:243], v[232:235], a[184:187], v[240:243]\n\t"
  "v_mfma_f32_16x16x32_bf16 v[240:243], v[232:235], a[188:191], v[240:243]\n\t"
  "LS0%=:\n\t"
  "s_cmp_gt_u32 s79, 32\n\t"
  "s_cbranch_scc0 LS1%=\n\t"
  "v_mfma_f32_16x16x32_bf16 v[240:243], v[236:239], a[48:51], v[240:243]\n\t"
  "s_cmp_gt_u32 s79, 36\n\t"
  "s_cbranch_scc0 LS1%=\n\t"
  "v_mfma_f32_16x16x32_bf16 v[240:243], v[236:239], a[52:55], v[240:243]\n\t"
  "s_cmp_gt_u32 s79, 40\n\t"
  "s_cbranch_scc0 LS1%=\n\t"
  "v_mfma_f32_16x16x32_bf16 v[240:243], v[236:239], a[56:59], v[240:243]\n\t"
  "s_cmp_gt_u32 s79, 44\n\t"
  "s_cbranch_scc0 LS1%=\n\t"
  "v_mfma_f32_16x16x32_bf16 v[240:243], v[236:239], a[60:63], v[240:243]\n\t"
  "s_cmp_gt_u32 s79, 48\n\t"
  "s_cbranch_scc0 LS1%=\n\t"
  "v_mfma_f32_16x16x32_bf16 v[240:243], v[236:239], a[176:179], v[240:243]\n\t"
  "v_mfma_f32_16x16x32_bf16 v[240:243], v[236:239], a[180:183], v[240:243]\n\t"
  "v_mfma_f32_16x16x32_bf16 v[240:243], v[236:239], a[184:187], v[240:243]\n\t"
  "v_mfma_f32_16x16x32_bf16 v[240:243], v[236:239], a[188:191], v[240:243]\n\t"
  "LS1%=:\n\t"
  "s_cmp_gt_u32 s80, 32\n\t"
  "s_cbranch_scc0 LS2%=\n\t"
  "v_mfma_f32_16x16x32_bf16 v[244:247], v[232:235], a[64:67], v[244:247]\n\t"
  "s_cmp_gt_u32 s80, 36\n\t"
  "s_cbranch_scc0 LS2%=\n\t"
  "v_mfma_f32_16x16x32_bf16 v[244:247], v[232:235], a[68:71], v[244:247]\n\t"
  "s_cmp_gt_u32 s80, 40\n\t"
  "s_cbranch_scc0 LS2%=\n\t"
  "v_mfma_f32_16x16x32_bf16 v[244:247], v[232:235], a[72:75], v[244:247]\n\t"
  "s_cmp_gt_u32 s80, 44\n\t"
  "s_cbranch_scc0 LS2%=\n\t"
  "v_mfma_f32_16x16x32_bf16 v[244:247], v[232:235], a[76:79], v[244:247]\n\t"
  "s_cmp_gt_u32 s80, 48\n\t"
  "s_cbranch_scc0 LS2%=\n\t"
  "v_mfma_f32_16x16x32_bf16 v[244:247], v[232:235], a[192:195], v[244:247]\n\t"
  "v_mfma_f32_16x16x32_bf16 v[244:247], v[232:235], a[196:199], v[244:247]\n\t"
  "v_mfma_f32_16x16x32_bf16 v[244:247], v[232:235], a[200:203], v[244:247]\n\t"
  "v_mfma_f32_16x16x32_bf16 v[244:247], v[232:235], a[204:207], v[244:247]\n\t"
  "LS2%=:\n\t"
  "s_cmp_gt_u32 s81, 32\n\t"
  "s_cbranch_scc0 LS3%=\n\t"
  "v_mfma_f32_16x16x32_bf16 v[244:247], v[236:239], a[80:83], v[244:247]\n\t"
  "s_cmp_gt_u32 s81, 36\n\t"
  "s_cbranch_scc0 LS3%=\n\t"
  "v_mfma_f32_16x16x32_bf16 v[244:247], v[236:239], a[84:87], v[244:247]\n\t"
  "s_cmp_gt_u32 s81, 40\n\t"
  "s_cbranch_scc0 LS3%=\n\t"
  "v_mfma_f32_16x16x32_bf16 v[244:247], v[236:239], a[88:91], v[244:247]\n\t"
  "s_cmp_gt_u32 s81, 44\n\t"
  "s_cbranch_scc0 LS3%=\n\t"
  "v_mfma_f32_16x16x32_bf16 v[244:247], v[236:239], a[92:95], v[244:247]\n\t"
  "s_cmp_gt_u32 s81, 48\n\t"
  "s_cbranch_scc0 LS3%=\n\t"
  "v_mfma_f32_16x16x32_bf16 v[244:247], v[236:239], a[192:195], v[244:247]\n\t"
  "v_mfma_f32_16x16x32_bf16 v[244:247], v[236:239], a[196:199], v[244:247]\n\t"
  "v_mfma_f32_16x16x32_bf16 v[244:247], v[236:239], a[200:203], v[244:247]\n\t"
  "v_mfma_f32_16x16x32_bf16 v[244:247], v[236:239], a[204:207], v[244:247]\n\t"
  "LS3%=:\n\t"
  "s_cmp_gt_u32 s82, 32\n\t"
  "s_cbranch_scc0 LS4%=\n\t"
  "v_mfma_f32_16x16x32_bf16 v[248:251], v[232:235], a[96:99], v[248:251]\n\t"
  "s_cmp_gt_u32 s82, 36\n\t"
  "s_cbranch_scc0 LS4%=\n\t"
  "v_mfma_f32_16x16x32_bf16 v[248:251], v[232:235], a[100:103], v[248:251]\n\t"
  "s_cmp_gt_u32 s82, 40\n\t"
  "s_cbranch_scc0 LS4%=\n\t"
  "v_mfma_f32_16x16x32_bf16 v[248:251], v[232:235], a[104:107], v[248:251]\n\t"
  "s_cmp_gt_u32 s82, 44\n\t"
  "s_cbranch_scc0 LS4%=\n\t"
  "v_mfma_f32_16x16x32_bf16 v[248:251], v[232:235], a[108:111], v[248:251]\n\t"
  "s_cmp_gt_u32 s82, 48\n\t"
  "s_cbranch_scc0 LS4%=\n\t"
  "v_mfma_f32_16x16x32_bf16 v[248:251], v[232:235], a[208:211], v[248:251]\n\t"
  "v_mfma_f32_16x16x32_bf16 v[248:251], v[232:235], a[212:215], v[248:251]\n\t"
  "v_mfma_f32_16x16x32_bf16 v[248:251], v[232:235], a[216:219], v[248:251]\n\t"
  "v_mfma_f32_16x16x32_bf16 v[248:251], v[232:235], a[220:223], v[248:251]\n\t"
  "LS4%=:\n\t"
  "buffer_load_dword v228, v215, %8, 0 offen sc1\n\t"
  "s_cmp_gt_u32 s83, 32\n\t"
  "s_cbranch_scc0 LS5%=\n\t"
  "v_mfma_f32_16x16x32_bf16 v[248:251], v[236:239], a[112:115], v[248:251]\n\t"
  "s_cmp_gt_u32 s83, 36\n\t"
  "s_cbranch_scc0 LS5%=\n\t"
  "v_mfma_f32_16x16x32_bf16 v[248:251], v[236:239], a[116:119], v[248:251]\n\t"
  "s_cmp_gt_u32 s83, 40\n\t"
  "s_cbranch_scc0 LS5%=\n\t"
  "v_mfma_f32_16x16x32_bf16 v[248:251], v[236:239], a[120:123], v[248:251]\n\t"
  "s_cmp_gt_u32 s83, 44\n\t"
  "s_cbranch_scc0 LS5%=\n\t"
  "v_mfma_f32_16x16x32_bf16 v[248:251], v[236:239], a[124:127], v[248:251]\n\t"
  "s_cmp_gt_u32 s83, 48\n\t"
  "s_cbranch_scc0 LS5%=\n\t"
  "v_mfma_f32_16x16x32_bf16 v[248:251], v[236:239], a[208:211], v[248:251]\n\t"
  "v_mfma_f32_16x16x32_bf16 v[248:251], v[236:239], a[212:215], v[248:251]\n\t"
  "v_mfma_f32_16x16x32_bf16 v[248:251], v[236:239], a[216:219], v[248:251]\n\t"
  "v_mfma_f32_16x16x32_bf16 v[248:251], v[236:239], a[220:223], v[248:251]\n\t"
  "LS5%=:\n\t"
  "s_cmp_gt_u32 s84, 32\n\t"
  "s_cbranch_scc0 LS6%=\n\t"
  "v_mfma_f32_16x16x32_bf16 v[252:255], v[232:235], a[128:131], v[252:255]\n\t"
  "s_cmp_gt_u32 s84, 36\n\t"
  "s_cbranch_scc0 LS6%=\n\t"
  "v_mfma_f32_16x16x32_bf16 v[252:255], v[232:235], a[132:135], v[252:255]\n\t"
  "s_cmp_gt_u32 s84, 40\n\t"
  "s_cbranch_scc0 LS6%=\n\t"
  "v_mfma_f32_16x16x32_bf16 v[252:255], v[232:235], a[136:139], v[252:255]\n\t"
  "s_cmp_gt_u32 s84, 44\n\t"
  "s_cbranch_scc0 LS6%=\n\t"
  "v_mfma_f32_16x16x32_bf16 v[252:255], v[232:235], a[140:143], v[252:255]\n\t"
  "s_cmp_gt_u32 s84, 48\n\t"
  "s_cbranch_scc0 LS6%=\n\t"
  "v_mfma_f32_16x16x32_bf16 v[252:255], v[232:235], a[224:227], v[252:255]\n\t"
  "v_mfma_f32_16x16x32_bf16 v[252:255], v[232:235], a[228:231], v[252:255]\n\t"
  "v_mfma_f32_16x16x32_bf16 v[252:255], v[232:235], a[232:235], v[252:255]\n\t"
  "v_mfma_f32_16x16x32_bf16 v[252:255], v[232:235], a[236:239], v[252:255]\n\t"
  "LS6%=:\n\t"
  "s_cmp_gt_u32 s85, 32\n\t"
  "s_cbranch_scc0 LS7%=\n\t"
  "v_mfma_f32_16x16x32_bf16 v[252:255], v[236:239], a[144:147], v[252:255]\n\t"
  "s_cmp_gt_u32 s85, 36\n\t"
  "s_cbranch_scc0 LS7%=\n\t"
  "v_mfma_f32_16x16x32_bf16 v[252:255], v[236:239], a[148:151], v[252:255]\n\t"
  "s_cmp_gt_u32 s85, 40\n\t"
  "s_cbranch_scc0 LS7%=\n\t"
  "v_mfma_f32_16x16x32_bf16 v[252:255], v[236:239], a[152:155], v[252:255]\n\t"
  "s_cmp_gt_u32 s85, 44\n\t"
  "s_cbranch_scc0 LS7%=\n\t"
  "v_mfma_f32_16x16x32_bf16 v[252:255], v[236:239], a[156:159], v[252:255]\n\t"
  "s_cmp_gt_u32 s85, 48\n\t"
  "s_cbranch_scc0 LS7%=\n\t"
  "v_mfma_f32_16x16x32_bf16 v[252:255], v[236:239], a[224:227], v[252:255]\n\t"
  "v_mfma_f32_16x16x32_bf16 v[252:255], v[236:239], a[228:231], v[252:255]\n\t"
  "v_mfma_f32_16x16x32_bf16 v[252:255], v[236:239], a[232:235], v[252:255]\n\t"
  "v_mfma_f32_16x16x32_bf16 v[252:255], v[236:239], a[236:239], v[252:255]\n\t"
  "LS7%=:\n\t"
  "s_cmp_gt_u32 s86, 32\n\t"
  "s_cbranch_scc0 LS8%=\n\t"
  "v_mfma_f32_16x16x32_bf16 v[192:195], v[232:235], a[160:163], v[192:195]\n\t"
  "s_cmp_gt_u32 s86, 36\n\t"
  "s_cbranch_scc0 LS8%=\n\t"
  "v_mfma_f32_16x16x32_bf16 v[192:195], v[232:235], a[164:167], v[192:195]\n\t"
  "s_cmp_gt_u32 s86, 40\n\t"
  "s_cbranch_scc0 LS8%=\n\t"
  "v_mfma_f32_16x16x32_bf16 v[192:195], v[232:235], a[168:171], v[192:195]\n\t"
  "s_cmp_gt_u32 s86, 44\n\t"
  "s_cbranch_scc0 LS8%=\n\t"
  "v_mfma_f32_16x16x32_bf16 v[192:195], v[232:235], a[172:175], v[192:195]\n\t"
  "s_cmp_gt_u32 s86, 48\n\t"
  "s_cbranch_scc0 LS8%=\n\t"
  "v_mfma_f32_16x16x32_bf16 v[192:195], v[232:235], a[240:243], v[192:195]\n\t"
  "v_mfma_f32_16x16x32_bf16 v[192:195], v[232:235], a[244:247], v[192:195]\n\t"
  "v_mfma_f32_16x16x32_bf16 v[192:195], v[232:235], a[248:251], v[192:195]\n\t"
  "v_mfma_f32_16x16x32_bf16 v[192:195], v[232:235], a[252:255], v[192:195]\n\t"
  "LS8%=:\n\t"
  "s_cmp_lg_u32 s87, 0\n\t"
  "s_cbranch_scc1 LTWO%=\n\t"
  "LBACK%=:\n\t"
  "s_waitcnt lgkmcnt(0)\n\t"
  "v_mfma_f32_16x16x32_bf16 v[128:131], v[198:201], a[16:19], v[128:131]\n\t"
  "ds_read_b128 a[32:35], v226 offset:32768\n\t"
  "ds_read_b128 a[36:39], v226 offset:33792\n\t"
  "ds_read_b128 a[40:43], v226 offset:34816\n\t"
  "v_mfma_f32_16x16x32_bf16 v[132:135], v[196:199], a[16:19], v[132:135]\n\t"
  "ds_read_b128 a[44:47], v226 offset:35840\n\t"
  "ds_read_b128 a[48:51], v226 offset:36864\n\t"
  "ds_read_b128 a[52:55], v226 offset:37888\n\t"
  "v_mfma_f32_16x16x32_bf16 v[136:139], v[198:201], a[20:23], v[136:139]\n\t"
  "ds_read_b128 a[56:59], v226 offset:38912\n\t"
  "ds_read_b128 a[60:63], v226 offset:39936\n\t"
  "ds_read_b128 a[64:67], v226 offset:40960\n\t"
  "v_mfma_f32_16x16x32_bf16 v[140:143], v[196:199], a[20:23], v[140:143]\n\t"
  "ds_read_b128 a[68:71], v226 offset:41984\n\t"
  "ds_read_b128 a[72:75], v226 offset:43008\n\t"
  "ds_read_b128 a[76:79], v226 offset:44032\n\t"
  "v_mfma_f32_16x16x32_bf16 v[144:147], v[198:201], a[24:27], v[144:147]\n\t"
  "ds_read_b128 a[80:83], v226 offset:45056\n\t"
  "ds_read_b128 a[84:87], v226 offset:46080\n\t"
  "ds_read_b128 a[88:91], v226 offset:47104\n\t"
  "v_mfma_f32_16x16x32_bf16 v[148:151], v[196:199], a[24:27], v[148:151]\n\t"
  "ds_read_b128 a[92:95], v226 offset:48128\n\t"
  "ds_read_b128 a[96:99], v226 offset:49152\n\t"
  "ds_read_b128 a[100:103], v226 offset:50176\n\t"
  "v_mfma_f32_16x16x32_bf16 v[152:155], v[198:201], a[28:31], v[152:155]\n\t"
  "ds_read_b128 a[104:107], v226 offset:51200\n\t"
  "ds_read_b128 a[108:111], v226 offset:52224\n\t"
  "ds_read_b128 a[112:115], v226 offset:53248\n\t"
  "v_mfma_f32_16x16x32_bf16 v[156:159], v[196:199], a[28:31], v[156:159]\n\t"
  "ds_read_b128 a[116:119], v226 offset:54272\n\t"
  "ds_read_b128 a[120:123], v226 offset:55296\n\t"
  "ds_read_b128 a[124:127], v226 offset:56320\n\t"
  "buffer_load_dword v216, v215, %8, 0 offen sc1\n\t"
  "v_mul_f32_e32 v240, v240, v202\n\t"
  "v_mul_f32_e32 v241, v241, v202\n\t"
  "v_mul_f32_e32 v242, v242, v202\n\t"
  "v_mul_f32_e32 v243, v243, v202\n\t"
  "v_cvt_pk_bf16_f32 v240, v240, v241\n\t"
  "v_cvt_pk_bf16_f32 v241, v242, v243\n\t"
  "ds_write_b64 v212, v[240:241] offset:1088\n\t"
  "v_mul_f32_e32 v244, v244, v203\n\t"
  "v_mul_f32_e32 v245, v245, v203\n\t"
  "v_mul_f32_e32 v246, v246, v203\n\t"
  "v_mul_f32_e32 v247, v247, v203\n\t"
  "v_cvt_pk_bf16_f32 v244, v244, v245\n\t"
  "v_cvt_pk_bf16_f32 v245, v246, v247\n\t"
  "ds_write_b64 v212, v[244:245] offset:1632\n\t"
  "v_mul_f32_e32 v248, v248, v204\n\t"
  "v_mul_f32_e32 v249, v249, v204\n\t"
  "v_mul_f32_e32 v250, v250, v204\n\t"
  "v_mul_f32_e32 v251, v251, v204\n\t"
  "v_cvt_pk_bf16_f32 v248, v248, v249\n\t"
  "v_cvt_pk_bf16_f32 v249, v250, v251\n\t"
  "ds_write_b64 v212, v[248:249] offset:2176\n\t"
  "v_mul_f32_e32 v252, v252, v205\n\t"
  "v_mul_f32_e32 v253, v253, v205\n\t"
  "v_mul_f32_e32 v254, v254, v205\n\t"
  "v_mul_f32_e32 v255, v255, v205\n\t"
  "v_cvt_pk_bf16_f32 v252, v252, v253\n\t"
  "v_cvt_pk_bf16_f32 v253, v254, v255\n\t"
  "ds_write_b64 v212, v[252:253] offset:2720\n\t"
  "v_mul_f32_e32 v192, v192, v206\n\t"
  "v_mul_f32_e32 v193, v193, v206\n\t"
  "v_mul_f32_e32 v194, v194, v206\n\t"
  "v_mul_f32_e32 v195, v195, v206\n\t"
  "v_cvt_pk_bf16_f32 v192, v192, v193\n\t"
  "v_cvt_pk_bf16_f32 v193, v194, v195\n\t"
  "ds_write_b64 v212, v[192:193] offset:3264\n\t"
  "s_mov_b32 s72, 0x100000\n\t"
  "s_waitcnt vmcnt(1)\n\t"
  "v_cmp_ne_u32_e32 vcc, 1, v228\n\t"
  "s_cbranch_vccz LGO%=\n\t"
  "LPOLL%=:\n\t"
  "s_waitcnt vmcnt(0)\n\t"
  "v_cmp_ne_u32_e32 vcc, 1, v216\n\t"
  "s_cbranch_vccz LGO%=\n\t"
  "buffer_load_dword v216, v215, %8, 0 offen sc1\n\t"
  "s_sub_u32 s72, s72, 1\n\t"
  "s_cbranch_scc0 LPOLL%=\n\t"
  "LGO%=:\n\t"
  "buffer_load_dwordx4 a[224:227], v224, %8, s43 offen sc1\n\t"
  "ds_read_b128 a[128:131], v226 offset:57344\n\t"
  "buffer_load_dwordx4 a[208:211], v231, %8, s44 offen offset:3072 sc1\n\t"
  "ds_read_b128 a[132:135], v226 offset:58368\n\t"
  "buffer_load_dwordx4 a[212:215], v231, %8, s45 offen offset:3072 sc1\n\t"
  "ds_read_b128 a[136:139], v226 offset:59392\n\t"
  "buffer_load_dwordx4 a[216:219], v231, %8, s46 offen offset:3072 sc1\n\t"
  "ds_read_b128 a[140:143], v226 offset:60416\n\t"
  "buffer_load_dwordx4 a[220:223], v231, %8, s47 offen offset:3072 sc1\n\t"
  "ds_read_b128 a[144:147], v226 offset:61440\n\t"
  "buffer_load_dwordx4 a[192:195], v231, %8, s44 offen offset:2048 sc1\n\t"
  "ds_read_b128 a[148:151], v226 offset:62464\n\t"
  "buffer_load_dwordx4 a[196:199], v231, %8, s45 offen offset:2048 sc1\n\t"
  "ds_read_b128 a[152:155], v226 offset:63488\n\t"
  "buffer_load_dwordx4 a[200:203], v231, %8, s46 offen offset:2048 sc1\n\t"
  "ds_read_b128 a[156:159], v226 offset:64512\n\t"
  "buffer_load_dwordx4 a[204:207], v231, %8, s47 offen offset:2048 sc1\n\t"
  "buffer_load_dwordx4 a[176:179], v231, %8, s44 offen offset:1024 sc1\n\t"
  "buffer_load_dwordx4 a[180:183], v231, %8, s45 offen offset:1024 sc1\n\t"
  "buffer_load_dwordx4 a[184:187], v231, %8, s46 offen offset:1024 sc1\n\t"
  "buffer_load_dwordx4 a[188:191], v231, %8, s47 offen offset:1024 sc1\n\t"
  "buffer_load_dwordx4 a[160:163], v231, %8, s44 offen offset:0 sc1\n\t"
  "buffer_load_dwordx4 a[164:167], v231, %8, s45 offen offset:0 sc1\n\t"
  "buffer_load_dwordx4 a[168:171], v231, %8, s46 offen offset:0 sc1\n\t"
  "buffer_load_dwordx4 a[172:175], v231, %8, s47 offen offset:0 sc1\n\t"
  "s_waitcnt vmcnt(16)\n\t"
  "ds_write_b128 v220, a[224:227] offset:0\n\t"
  "ds_read_b128 v[240:243], v221 offset:0\n\t"
  "ds_read_b128 v[244:247], v221 offset:64\n\t"
  "ds_read_b128 v[248:251], v221 offset:128\n\t"
  "ds_read_b128 v[252:255], v221 offset:192\n\t"
  "s_waitcnt lgkmcnt(0)\n\t"
  "v_mfma_f32_16x16x32_bf16 v[160:163], a[32:35], v[240:243], v[160:163]\n\t"
  "v_mfma_f32_16x16x32_bf16 v[164:167], a[48:51], v[240:243], v[164:167]\n\t"
  "v_mfma_f32_16x16x32_bf16 v[168:171], a[64:67], v[240:243], v[168:171]\n\t"
  "v_mfma_f32_16x16x32_bf16 v[172:175], a[80:83], v[240:243], v[172:175]\n\t"
  "v_mfma_f32_16x16x32_bf16 v[176:179], a[96:99], v[240:243], v[176:179]\n\t"
  "v_mfma_f32_16x16x32_bf16 v[180:183], a[112:115], v[240:243], v[180:183]\n\t"
  "v_mfma_f32_16x16x32_bf16 v[184:187], a[128:131], v[240:243], v[184:187]\n\t"
  "v_mfma_f32_16x16x32_bf16 v[188:191], a[144:147], v[240:243], v[188:191]\n\t"
  "v_mfma_f32_16x16x32_bf16 v[160:163], a[36:39], v[244:247], v[160:163]\n\t"
  "v_mfma_f32_16x16x32_bf16 v[164:167], a[52:55], v[244:247], v[164:167]\n\t"
  "v_mfma_f32_16x16x32_bf16 v[168:171], a[68:71], v[244:247], v[168:171]\n\t"
  "v_mfma_f32_16x16x32_bf16 v[172:175], a[84:87], v[244:247], v[172:175]\n\t"
  "v_mfma_f32_16x16x32_bf16 v[176:179], a[100:103], v[244:247], v[176:179]\n\t"
  "v_mfma_f32_16x16x32_bf16 v[180:183], a[116:119], v[244:247], v[180:183]\n\t"
  "v_mfma_f32_16x16x32_bf16 v[184:187], a[132:135], v[244:247], v[184:187]\n\t"
  "v_mfma_f32_16x16x32_bf16 v[188:191], a[148:151], v[244:247], v[188:191]\n\t"
  "v_mfma_f32_16x16x32_bf16 v[160:163], a[40:43], v[248:251], v[160:163]\n\t"
  "v_mfma_f32_16x16x32_bf16 v[164:167], a[56:59], v[248:251], v[164:167]\n\t"
  "v_mfma_f32_16x16x32_bf16 v[168:171], a[72:75], v[248:251], v[168:171]\n\t"
  "v_mfma_f32_16x16x32_bf16 v[172:175], a[88:91], v[248:251], v[172:175]\n\t"
  "v_mfma_f32_16x16x32_bf16 v[176:179], a[104:107], v[248:251], v[176:179]\n\t"
  "v_mfma_f32_16x16x32_bf16 v[180:183], a[120:123], v[248:251], v[180:183]\n\t"
  "v_mfma_f32_16x16x32_bf16 v[184:187], a[136:139], v[248:251], v[184:187]\n\t"
  "v_mfma_f32_16x16x32_bf16 v[188:191], a[152:155], v[248:251], v[188:191]\n\t"
  "v_mfma_f32_16x16x32_bf16 v[160:163], a[44:47], v[252:255], v[160:163]\n\t"
  "v_mfma_f32_16x16x32_bf16 v[164:167], a[60:63], v[252:255], v[164:167]\n\t"
  "v_mfma_f32_16x16x32_bf16 v[168:171], a[76:79], v[252:255], v[168:171]\n\t"
  "v_mfma_f32_16x16x32_bf16 v[172:175], a[92:95], v[252:255], v[172:175]\n\t"
  "v_mfma_f32_16x16x32_bf16 v[176:179], a[108:111], v[252:255], v[176:179]\n\t"
  "v_mfma_f32_16x16x32_bf16 v[180:183], a[124:127], v[252:255], v[180:183]\n\t"
  "v_mfma_f32_16x16x32_bf16 v[184:187], a[140:143], v[252:255], v[184:187]\n\t"
  "v_mfma_f32_16x16x32_bf16 v[188:191], a[156:159], v[252:255], v[188:191]\n\t"
  "s_waitcnt vmcnt(12)\n\t"
  "v_mfma_f32_16x16x32_bf16 v[152:155], v[198:201], a[208:211], v[152:155]\n\t"
  "v_mfma_f32_16x16x32_bf16 v[156:159], v[196:199], a[208:211], v[156:159]\n\t"
  "v_mfma_f32_16x16x32_bf16 v[152:155], v[198:201], a[212:215], v[152:155]\n\t"
  "v_mfma_f32_16x16x32_bf16 v[156:159], v[196:199], a[212:215], v[156:159]\n\t"
  "v_mfma_f32_16x16x32_bf16 v[152:155], v[198:201], a[216:219], v[152:155]\n\t"
  "v_mfma_f32_16x16x32_bf16 v[156:159], v[196:199], a[216:219], v[156:159]\n\t"
  "v_mfma_f32_16x16x32_bf16 v[152:155], v[198:201], a[220:223], v[152:155]\n\t"
  "v_mfma_f32_16x16x32_bf16 v[156:159], v[196:199], a[220:223], v[156:159]\n\t"
  "s_waitcnt vmcnt(8)\n\t"
  "v_mfma_f32_16x16x32_bf16 v[144:147], v[198:201], a[192:195], v[144:147]\n\t"
  "v_mfma_f32_16x16x32_bf16 v[148:151], v[196:199], a[192:195], v[148:151]\n\t"
  "v_mfma_f32_16x16x32_bf16 v[144:147], v[198:201], a[196:199], v[144:147]\n\t"
  "s_nop 3\n\t"
  "v_rcp_f32_e32 v193, v159\n\t"
  "s_nop 0\n\t"
  "v_mov_b32_e32 v192, v193\n\t"
  "v_mfma_f32_16x16x32_bf16 v[148:151], v[196:199], a[196:199], v[148:151]\n\t"
  "s_nop 1\n\t"
  "v_permlane32_swap_b32_e32 v193, v192\n\t"
  "s_nop 1\n\t"
  "v_mfma_f32_16x16x32_bf16 v[144:147], v[198:201], a[200:203], v[144:147]\n\t"
  "v_mov_b32_e32 v193, v192\n\t"
  "s_nop 1\n\t"
  "v_permlane16_swap_b32_e32 v193, v192\n\t"
  "v_mfma_f32_16x16x32_bf16 v[148:151], v[196:199], a[200:203], v[148:151]\n\t"
  "s_nop 1\n\t"
  "v_pk_mul_f32 v[152:153], v[152:153], v[192:193] op_sel_hi:[1,0]\n\t"
  "v_pk_mul_f32 v[154:155], v[154:155], v[192:193] op_sel_hi:[1,0]\n\t"
  "v_mfma_f32_16x16x32_bf16 v[144:147], v[198:201], a[204:207], v[144:147]\n\t"
  "v_pk_mul_f32 v[156:157], v[156:157], v[192:193] op_sel_hi:[1,0]\n\t"
  "v_pk_mul_f32 v[158:159], v[158:159], v[192:193] op_sel_hi:[1,0]\n\t"
  "v_cvt_pk_bf16_f32 v152, v152, v153\n\t"
  "v_mfma_f32_16x16x32_bf16 v[148:151], v[196:199], a[204:207], v[148:151]\n\t"
  "v_cvt_pk_bf16_f32 v153, v154, v155\n\t"
  "v_cvt_pk_bf16_f32 v154, v156, v157\n\t"
  "v_cvt_pk_bf16_f32 v155, v158, v159\n\t"
  "s_waitcnt vmcnt(4)\n\t"
  "v_mfma_f32_16x16x32_bf16 v[136:139], v[198:201], a[176:179], v[136:139]\n\t"
  "v_mfma_f32_16x16x32_bf16 v[140:143], v[196:199], a[176:179], v[140:143]\n\t"
  "v_mfma_f32_16x16x32_bf16 v[136:139], v[198:201], a[180:183], v[136:139]\n\t"
  "v_pk_mul_f32 v[144:145], v[144:145], v[192:193] op_sel_hi:[1,0]\n\t"
  "v_pk_mul_f32 v[146:147], v[146:147], v[192:193] op_sel_hi:[1,0]\n\t"
  "v_mfma_f32_16x16x32_bf16 v[140:143], v[196:199], a[180:183], v[140:143]\n\t"
  "v_pk_mul_f32 v[148:149], v[148:149], v[192:193] op_sel_hi:[1,0]\n\t"
  "v_pk_mul_f32 v[150:151], v[150:151], v[192:193] op_sel_hi:[1,0]\n\t"
  "v_mfma_f32_16x16x32_bf16 v[136:139], v[198:201], a[184:187], v[136:139]\n\t"
  "v_cvt_pk_bf16_f32 v144, v144, v145\n\t"
  "v_cvt_pk_bf16_f32 v145, v146, v147\n\t"
  "v_mfma_f32_16x16x32_bf16 v[140:143], v[196:199], a[184:187], v[140:143]\n\t"
  "v_cvt_pk_bf16_f32 v146, v148, v149\n\t"
  "v_cvt_pk_bf16_f32 v147, v150, v151\n\t"
  "v_mfma_f32_16x16x32_bf16 v[136:139], v[198:201], a[188:191], v[136:139]\n\t"
  "v_mfma_f32_16x16x32_bf16 v[140:143], v[196:199], a[188:191], v[140:143]\n\t"
  "v_mfma_f32_16x16x32_bf16 v[160:163], v[12:15], v[152:155], v[160:163]\n\t"
  "v_mfma_f32_16x16x32_bf16 v[164:167], v[28:31], v[152:155], v[164:167]\n\t"
  "v_mfma_f32_16x16x32_bf16 v[168:171], v[44:47], v[152:155], v[168:171]\n\t"
  "v_mfma_f32_16x16x32_bf16 v[172:175], v[60:63], v[152:155], v[172:175]\n\t"
  "v_mfma_f32_16x16x32_bf16 v[176:179], v[76:79], v[152:155], v[176:179]\n\t"
  "v_mfma_f32_16x16x32_bf16 v[180:183], v[92:95], v[152:155], v[180:183]\n\t"
  "v_mfma_f32_16x16x32_bf16 v[184:187], v[108:111], v[152:155], v[184:187]\n\t"
  "v_mfma_f32_16x16x32_bf16 v[188:191], v[124:127], v[152:155], v[188:191]\n\t"
  "v_mfma_f32_16x16x32_bf16 v[160:163], v[8:11], v[144:147], v[160:163]\n\t"
  "v_pk_mul_f32 v[136:137], v[136:137], v[192:193] op_sel_hi:[1,0]\n\t"
  "v_pk_mul_f32 v[138:139], v[138:139], v[192:193] op_sel_hi:[1,0]\n\t"
  "v_mfma_f32_16x16x32_bf16 v[164:167], v[24:27], v[144:147], v[164:167]\n\t"
  "v_pk_mul_f32 v[140:141], v[140:141], v[192:193] op_sel_hi:[1,0]\n\t"
  "v_pk_mul_f32 v[142:143], v[142:143], v[192:193] op_sel_hi:[1,0]\n\t"
  "v_mfma_f32_16x16x32_bf16 v[168:171], v[40:43], v[144:147], v[168:171]\n\t"
  "v_cvt_pk_bf16_f32 v136, v136, v137\n\t"
  "v_cvt_pk_bf16_f32 v137, v138, v139\n\t"
  "v_mfma_f32_16x16x32_bf16 v[172:175], v[56:59], v[144:147], v[172:175]\n\t"
  "v_cvt_pk_bf16_f32 v138, v140, v141\n\t"
  "v_cvt_pk_bf16_f32 v139, v142, v143\n\t"
  "v_mfma_f32_16x16x32_bf16 v[176:179], v[72:75], v[144:147], v[176:179]\n\t"
  "v_mfma_f32_16x16x32_bf16 v[180:183], v[88:91], v[144:147], v[180:183]\n\t"
  "v_mfma_f32_16x16x32_bf16 v[184:187], v[104:107], v[144:147], v[184:187]\n\t"
  "v_mfma_f32_16x16x32_bf16 v[188:191], v[120:123], v[144:147], v[188:191]\n\t"
  "s_waitcnt vmcnt(0)\n\t"
  "v_mfma_f32_16x16x32_bf16 v[128:131], v[198:201], a[160:163], v[128:131]\n\t"
  "v_mfma_f32_16x16x32_bf16 v[132:135], v[196:199], a[160:163], v[132:135]\n\t"
  "v_mfma_f32_16x16x32_bf16 v[128:131], v[198:201], a[164:167], v[128:131]\n\t"
  "v_mfma_f32_16x16x32_bf16 v[132:135], v[196:199], a[164:167], v[132:135]\n\t"
  "v_mfma_f32_16x16x32_bf16 v[128:131], v[198:201], a[168:171], v[128:131]\n\t"
  "v_mfma_f32_16x16x32_bf16 v[132:135], v[196:199], a[168:171], v[132:135]\n\t"
  "v_mfma_f32_16x16x32_bf16 v[128:131], v[198:201], a[172:175], v[128:131]\n\t"
  "v_mfma_f32_16x16x32_bf16 v[132:135], v[196:199], a[172:175], v[132:135]\n\t"
  "v_mfma_f32_16x16x32_bf16 v[160:163], v[4:7], v[136:139], v[160:163]\n\t"
  "v_mfma_f32_16x16x32_bf16 v[164:167], v[20:23], v[136:139], v[164:167]\n\t"
  "v_mfma_f32_16x16x32_bf16 v[168:171], v[36:39], v[136:139], v[168:171]\n\t"
  "s_nop 3\n\t"
  "v_pk_mul_f32 v[128:129], v[128:129], v[192:193] op_sel_hi:[1,0]\n\t"
  "v_pk_mul_f32 v[130:131], v[130:131], v[192:193] op_sel_hi:[1,0]\n\t"
  "v_mfma_f32_16x16x32_bf16 v[172:175], v[52:55], v[136:139], v[172:175]\n\t"
  "v_pk_mul_f32 v[132:133], v[132:133], v[192:193] op_sel_hi:[1,0]\n\t"
  "v_pk_mul_f32 v[134:135], v[134:135], v[192:193] op_sel_hi:[1,0]\n\t"
  "v_mfma_f32_16x16x32_bf16 v[176:179], v[68:71], v[136:139], v[176:179]\n\t"
  "v_cvt_pk_bf16_f32 v128, v128, v129\n\t"
  "v_cvt_pk_bf16_f32 v129, v130, v131\n\t"
  "v_mfma_f32_16x16x32_bf16 v[180:183], v[84:87], v[136:139], v[180:183]\n\t"
  "v_cvt_pk_bf16_f32 v130, v132, v133\n\t"
  "v_cvt_pk_bf16_f32 v131, v134, v135\n\t"
  "v_mfma_f32_16x16x32_bf16 v[184:187], v[100:103], v[136:139], v[184:187]\n\t"
  "v_mfma_f32_16x16x32_bf16 v[188:191], v[116:119], v[136:139], v[188:191]\n\t"
  "v_mfma_f32_16x16x32_bf16 v[160:163], v[0:3], v[128:131], v[160:163]\n\t"
  "v_mfma_f32_16x16x32_bf16 v[164:167], v[16:19], v[128:131], v[164:167]\n\t"
  "v_mfma_f32_16x16x32_bf16 v[168:171], v[32:35], v[128:131], v[168:171]\n\t"
  "v_mfma_f32_16x16x32_bf16 v[172:175], v[48:51], v[128:131], v[172:175]\n\t"
  "v_mfma_f32_16x16x32_bf16 v[176:179], v[64:67], v[128:131], v[176:179]\n\t"
  "s_nop 3\n\t"
  "v_cvt_pk_bf16_f32 v160, v160, v161\n\t"
  "v_cvt_pk_bf16_f32 v161, v162, v163\n\t"
  "v_cvt_pk_bf16_f32 v162, v164, v165\n\t"
  "v_mfma_f32_16x16x32_bf16 v[180:183], v[80:83], v[128:131], v[180:183]\n\t"
  "v_cvt_pk_bf16_f32 v163, v166, v167\n\t"
  "buffer_store_dwordx4 v[160:163], v230, %10, 0 offen offset:0 sc1\n\t"
  "v_cvt_pk_bf16_f32 v168, v168, v169\n\t"
  "v_mfma_f32_16x16x32_bf16 v[184:187], v[96:99], v[128:131], v[184:187]\n\t"
  "v_cvt_pk_bf16_f32 v169, v170, v171\n\t"
  "v_cvt_pk_bf16_f32 v170, v172, v173\n\t"
  "v_cvt_pk_bf16_f32 v171, v174, v175\n\t"
  "v_mfma_f32_16x16x32_bf16 v[188:191], v[112:115], v[128:131], v[188:191]\n\t"
  "buffer_store_dwordx4 v[168:171], v230, %10, 0 offen offset:64 sc1\n\t"
  "v_cvt_pk_bf16_f32 v176, v176, v177\n\t"
  "v_cvt_pk_bf16_f32 v177, v178, v179\n\t"
  "v_cvt_pk_bf16_f32 v178, v180, v181\n\t"
  "v_cvt_pk_bf16_f32 v179, v182, v183\n\t"
  "buffer_store_dwordx4 v[176:179], v230, %10, 0 offen offset:128 sc1\n\t"
  "v_cvt_pk_bf16_f32 v184, v184, v185\n\t"
  "v_cvt_pk_bf16_f32 v185, v186, v187\n\t"
  "v_cvt_pk_bf16_f32 v186, v188, v189\n\t"
  "v_cvt_pk_bf16_f32 v187, v190, v191\n\t"
  "buffer_store_dwordx4 v[184:187], v230, %10, 0 offen offset:192 sc1\n\t"
  "s_endpgm\n\t"
  "LTWO%=:\n\t"
  "s_bitcmp1_b32 s87, 4\n\t"
  "s_cbranch_scc0 LT0%=\n\t"
  "ds_read_b64 v[208:209], v229 offset:152\n\t"
  "s_waitcnt lgkmcnt(0)\n\t"
  "v_mad_u32_u16 v217, v208, s90, v227\n\t"
  "buffer_load_dwordx4 a[176:179], v217, %7, 0 offen offset:1024\n\t"
  "v_mad_u32_u16 v218, v208, s90, v227 op_sel:[1,0,0,0]\n\t"
  "buffer_load_dwordx4 a[180:183], v218, %7, 0 offen offset:1024\n\t"
  "v_mad_u32_u16 v217, v209, s90, v227\n\t"
  "buffer_load_dwordx4 a[184:187], v217, %7, 0 offen offset:1024\n\t"
  "v_mad_u32_u16 v218, v209, s90, v227 op_sel:[1,0,0,0]\n\t"
  "buffer_load_dwordx4 a[188:191], v218, %7, 0 offen offset:1024\n\t"
  "s_waitcnt vmcnt(0)\n\t"
  "v_mfma_f32_16x16x32_bf16 v[240:243], v[236:239], a[176:179], v[240:243]\n\t"
  "v_mfma_f32_16x16x32_bf16 v[240:243], v[236:239], a[180:183], v[240:243]\n\t"
  "v_mfma_f32_16x16x32_bf16 v[240:243], v[236:239], a[184:187], v[240:243]\n\t"
  "v_mfma_f32_16x16x32_bf16 v[240:243], v[236:239], a[188:191], v[240:243]\n\t"
  "LT0%=:\n\t"
  "s_bitcmp1_b32 s87, 6\n\t"
  "s_cbranch_scc0 LT1%=\n\t"
  "ds_read_b64 v[208:209], v229 offset:408\n\t"
  "s_waitcnt lgkmcnt(0)\n\t"
  "v_mad_u32_u16 v217, v208, s90, v227\n\t"
  "buffer_load_dwordx4 a[192:195], v217, %7, 0 offen offset:1024\n\t"
  "v_mad_u32_u16 v218, v208, s90, v227 op_sel:[1,0,0,0]\n\t"
  "buffer_load_dwordx4 a[196:199], v218, %7, 0 offen offset:1024\n\t"
  "v_mad_u32_u16 v217, v209, s90, v227\n\t"
  "buffer_load_dwordx4 a[200:203], v217, %7, 0 offen offset:1024\n\t"
  "v_mad_u32_u16 v218, v209, s90, v227 op_sel:[1,0,0,0]\n\t"
  "buffer_load_dwordx4 a[204:207], v218, %7, 0 offen offset:1024\n\t"
  "s_waitcnt vmcnt(0)\n\t"
  "v_mfma_f32_16x16x32_bf16 v[244:247], v[236:239], a[192:195], v[244:247]\n\t"
  "v_mfma_f32_16x16x32_bf16 v[244:247], v[236:239], a[196:199], v[244:247]\n\t"
  "v_mfma_f32_16x16x32_bf16 v[244:247], v[236:239], a[200:203], v[244:247]\n\t"
  "v_mfma_f32_16x16x32_bf16 v[244:247], v[236:239], a[204:207], v[244:247]\n\t"
  "LT1%=:\n\t"
  "s_bitcmp1_b32 s87, 8\n\t"
  "s_cbranch_scc0 LT2%=\n\t"
  "ds_read_b64 v[208:209], v229 offset:664\n\t"
  "s_waitcnt lgkmcnt(0)\n\t"
  "v_mad_u32_u16 v217, v208, s90, v227\n\t"
  "buffer_load_dwordx4 a[208:211], v217, %7, 0 offen offset:1024\n\t"
  "v_mad_u32_u16 v218, v208, s90, v227 op_sel:[1,0,0,0]\n\t"
  "buffer_load_dwordx4 a[212:215], v218, %7, 0 offen offset:1024\n\t"
  "v_mad_u32_u16 v217, v209, s90, v227\n\t"
  "buffer_load_dwordx4 a[216:219], v217, %7, 0 offen offset:1024\n\t"
  "v_mad_u32_u16 v218, v209, s90, v227 op_sel:[1,0,0,0]\n\t"
  "buffer_load_dwordx4 a[220:223], v218, %7, 0 offen offset:1024\n\t"
  "s_waitcnt vmcnt(0)\n\t"
  "v_mfma_f32_16x16x32_bf16 v[248:251], v[236:239], a[208:211], v[248:251]\n\t"
  "v_mfma_f32_16x16x32_bf16 v[248:251], v[236:239], a[212:215], v[248:251]\n\t"
  "v_mfma_f32_16x16x32_bf16 v[248:251], v[236:239], a[216:219], v[248:251]\n\t"
  "v_mfma_f32_16x16x32_bf16 v[248:251], v[236:239], a[220:223], v[248:251]\n\t"
  "LT2%=:\n\t"
  "s_bitcmp1_b32 s87, 10\n\t"
  "s_cbranch_scc0 LT3%=\n\t"
  "ds_read_b64 v[208:209], v229 offset:920\n\t"
  "s_waitcnt lgkmcnt(0)\n\t"
  "v_mad_u32_u16 v217, v208, s90, v227\n\t"
  "buffer_load_dwordx4 a[224:227], v217, %7, 0 offen offset:1024\n\t"
  "v_mad_u32_u16 v218, v208, s90, v227 op_sel:[1,0,0,0]\n\t"
  "buffer_load_dwordx4 a[228:231], v218, %7, 0 offen offset:1024\n\t"
  "v_mad_u32_u16 v217, v209, s90, v227\n\t"
  "buffer_load_dwordx4 a[232:235], v217, %7, 0 offen offset:1024\n\t"
  "v_mad_u32_u16 v218, v209, s90, v227 op_sel:[1,0,0,0]\n\t"
  "buffer_load_dwordx4 a[236:239], v218, %7, 0 offen offset:1024\n\t"
  "s_waitcnt vmcnt(0)\n\t"
  "v_mfma_f32_16x16x32_bf16 v[252:255], v[236:239], a[224:227], v[252:255]\n\t"
  "v_mfma_f32_16x16x32_bf16 v[252:255], v[236:239], a[228:231], v[252:255]\n\t"
  "v_mfma_f32_16x16x32_bf16 v[252:255], v[236:239], a[232:235], v[252:255]\n\t"
  "v_mfma_f32_16x16x32_bf16 v[252:255], v[236:239], a[236:239], v[252:255]\n\t"
  "LT3%=:\n\t"
  "s_branch LBACK%=\n\t"
  "LSLOW%=:\n\t"
  "v_mfma_f32_16x16x32_bf16 v[0:3], a[64:67], v[176:179], v[0:3]\n\t"
  "s_mov_b32 s63, s55\n\t"
  "v_exp_f32_e32 v157, v157\n\t"
  "v_mfma_f32_16x16x32_bf16 v[4:7], a[68:71], v[176:179], v[4:7]\n\t"
  "v_cvt_pk_bf16_f32 v190, v156, v157\n\t"
  "v_mfma_f32_16x16x32_bf16 v[8:11], a[72:75], v[176:179], v[8:11]\n\t"
  "v_exp_f32_e32 v158, v158\n\t"
  "v_mfma_f32_16x16x32_bf16 v[12:15], a[76:79], v[176:179], v[12:15]\n\t"
  "s_waitcnt lgkmcnt(0)\n\t"
  "v_exp_f32_e32 v159, v159\n\t"
  "v_mfma_f32_16x16x32_bf16 v[16:19], a[80:83], v[176:179], v[16:19]\n\t"
  "v_cvt_pk_bf16_f32 v191, v158, v159\n\t"
  "v_mfma_f32_16x16x32_bf16 v[20:23], a[84:87], v[176:179], v[20:23]\n\t"
  "v_cvt_pk_bf16_f32 v0, v0, v1\n\t"
  "v_cvt_pk_bf16_f32 v1, v2, v3\n\t"
  "v_mfma_f32_16x16x32_bf16 v[24:27], a[88:91], v[176:179], v[24:27]\n\t"
  "v_cvt_pk_bf16_f32 v2, v4, v5\n\t"
  "v_cvt_pk_bf16_f32 v3, v6, v7\n\t"
  "buffer_store_dwordx4 v[0:3], v220, %8, s63 offen offset:0 sc1\n\t"
  "v_mfma_f32_16x16x32_bf16 v[28:31], a[92:95], v[176:179], v[28:31]\n\t"
  "v_cvt_pk_bf16_f32 v8, v8, v9\n\t"
  "v_cvt_pk_bf16_f32 v9, v10, v11\n\t"
  "v_mfma_f32_16x16x32_bf16 v[32:35], a[64:67], v[180:183], v[32:35]\n\t"
  "v_cvt_pk_bf16_f32 v10, v12, v13\n\t"
  "v_cvt_pk_bf16_f32 v11, v14, v15\n\t"
  "buffer_store_dwordx4 v[8:11], v220, %8, s63 offen offset:1024 sc1\n\t"
  "v_mfma_f32_16x16x32_bf16 v[36:39], a[68:71], v[180:183], v[36:39]\n\t"
  "v_cvt_pk_bf16_f32 v16, v16, v17\n\t"
  "v_cvt_pk_bf16_f32 v17, v18, v19\n\t"
  "v_mfma_f32_16x16x32_bf16 v[40:43], a[72:75], v[180:183], v[40:43]\n\t"
  "v_cvt_pk_bf16_f32 v18, v20, v21\n\t"
  "v_cvt_pk_bf16_f32 v19, v22, v23\n\t"
  "buffer_store_dwordx4 v[16:19], v220, %8, s63 offen offset:2048 sc1\n\t"
  "v_mfma_f32_16x16x32_bf16 v[44:47], a[76:79], v[180:183], v[44:47]\n\t"
  "v_cvt_pk_bf16_f32 v24, v24, v25\n\t"
  "v_cvt_pk_bf16_f32 v25, v26, v27\n\t"
  "v_mfma_f32_16x16x32_bf16 v[48:51], a[80:83], v[180:183], v[48:51]\n\t"
  "v_cvt_pk_bf16_f32 v26, v28, v29\n\t"
  "v_cvt_pk_bf16_f32 v27, v30, v31\n\t"
  "buffer_store_dwordx4 v[24:27], v220, %8, s63 offen offset:3072 sc1\n\t"
  "s_add_u32 s63, s63, 0x1000\n\t"
  "v_mfma_f32_16x16x32_bf16 v[52:55], a[84:87], v[180:183], v[52:55]\n\t"
  "v_cvt_pk_bf16_f32 v32, v32, v33\n\t"
  "v_cvt_pk_bf16_f32 v33, v34, v35\n\t"
  "v_mfma_f32_16x16x32_bf16 v[56:59], a[88:91], v[180:183], v[56:59]\n\t"
  "v_cvt_pk_bf16_f32 v34, v36, v37\n\t"
  "v_cvt_pk_bf16_f32 v35, v38, v39\n\t"
  "buffer_store_dwordx4 v[32:35], v221, %8, s63 offen offset:0 sc1\n\t"
  "v_mfma_f32_16x16x32_bf16 v[60:63], a[92:95], v[180:183], v[60:63]\n\t"
  "v_cvt_pk_bf16_f32 v40, v40, v41\n\t"
  "v_cvt_pk_bf16_f32 v41, v42, v43\n\t"
  "v_mfma_f32_16x16x32_bf16 v[64:67], a[64:67], v[184:187], v[64:67]\n\t"
  "v_cvt_pk_bf16_f32 v42, v44, v45\n\t"
  "v_cvt_pk_bf16_f32 v43, v46, v47\n\t"
  "buffer_store_dwordx4 v[40:43], v221, %8, s63 offen offset:1024 sc1\n\t"
  "v_mfma_f32_16x16x32_bf16 v[68:71], a[68:71], v[184:187], v[68:71]\n\t"
  "v_cvt_pk_bf16_f32 v48, v48, v49\n\t"
  "v_cvt_pk_bf16_f32 v49, v50, v51\n\t"
  "v_mfma_f32_16x16x32_bf16 v[72:75], a[72:75], v[184:187], v[72:75]\n\t"
  "v_cvt_pk_bf16_f32 v50, v52, v53\n\t"
  "v_cvt_pk_bf16_f32 v51, v54, v55\n\t"
  "buffer_store_dwordx4 v[48:51], v221, %8, s63 offen offset:2048 sc1\n\t"
  "v_mfma_f32_16x16x32_bf16 v[76:79], a[76:79], v[184:187], v[76:79]\n\t"
  "v_cvt_pk_bf16_f32 v56, v56, v57\n\t"
  "v_cvt_pk_bf16_f32 v57, v58, v59\n\t"
  "v_mfma_f32_16x16x32_bf16 v[80:83], a[80:83], v[184:187], v[80:83]\n\t"
  "v_cvt_pk_bf16_f32 v58, v60, v61\n\t"
  "v_cvt_pk_bf16_f32 v59, v62, v63\n\t"
  "buffer_store_dwordx4 v[56:59], v221, %8, s63 offen offset:3072 sc1\n\t"
  "s_add_u32 s63, s63, 0x1000\n\t"
  "v_mfma_f32_16x16x32_bf16 v[84:87], a[84:87], v[184:187], v[84:87]\n\t"
  "v_cvt_pk_bf16_f32 v64, v64, v65\n\t"
  "v_cvt_pk_bf16_f32 v65, v66, v67\n\t"
  "v_mfma_f32_16x16x32_bf16 v[88:91], a[88:91], v[184:187], v[88:91]\n\t"
  "v_cvt_pk_bf16_f32 v66, v68, v69\n\t"
  "v_cvt_pk_bf16_f32 v67, v70, v71\n\t"
  "buffer_store_dwordx4 v[64:67], v222, %8, s63 offen offset:0 sc1\n\t"
  "v_mfma_f32_16x16x32_bf16 v[92:95], a[92:95], v[184:187], v[92:95]\n\t"
  "v_cvt_pk_bf16_f32 v72, v72, v73\n\t"
  "v_cvt_pk_bf16_f32 v73, v74, v75\n\t"
  "v_mfma_f32_16x16x32_bf16 v[96:99], a[64:67], v[188:191], v[96:99]\n\t"
  "v_cvt_pk_bf16_f32 v74, v76, v77\n\t"
  "v_cvt_pk_bf16_f32 v75, v78, v79\n\t"
  "buffer_store_dwordx4 v[72:75], v222, %8, s63 offen offset:1024 sc1\n\t"
  "v_mfma_f32_16x16x32_bf16 v[100:103], a[68:71], v[188:191], v[100:103]\n\t"
  "v_cvt_pk_bf16_f32 v80, v80, v81\n\t"
  "v_cvt_pk_bf16_f32 v81, v82, v83\n\t"
  "v_mfma_f32_16x16x32_bf16 v[104:107], a[72:75], v[188:191], v[104:107]\n\t"
  "v_cvt_pk_bf16_f32 v82, v84, v85\n\t"
  "v_cvt_pk_bf16_f32 v83, v86, v87\n\t"
  "buffer_store_dwordx4 v[80:83], v222, %8, s63 offen offset:2048 sc1\n\t"
  "v_mfma_f32_16x16x32_bf16 v[108:111], a[76:79], v[188:191], v[108:111]\n\t"
  "v_cvt_pk_bf16_f32 v88, v88, v89\n\t"
  "v_cvt_pk_bf16_f32 v89, v90, v91\n\t"
  "v_mfma_f32_16x16x32_bf16 v[112:115], a[80:83], v[188:191], v[112:115]\n\t"
  "v_cvt_pk_bf16_f32 v90, v92, v93\n\t"
  "v_cvt_pk_bf16_f32 v91, v94, v95\n\t"
  "buffer_store_dwordx4 v[88:91], v222, %8, s63 offen offset:3072 sc1\n\t"
  "s_add_u32 s63, s63, 0x1000\n\t"
  "v_mfma_f32_16x16x32_bf16 v[116:119], a[84:87], v[188:191], v[116:119]\n\t"
  "v_cvt_pk_bf16_f32 v96, v96, v97\n\t"
  "v_cvt_pk_bf16_f32 v97, v98, v99\n\t"
  "v_mfma_f32_16x16x32_bf16 v[120:123], a[88:91], v[188:191], v[120:123]\n\t"
  "v_cvt_pk_bf16_f32 v98, v100, v101\n\t"
  "v_cvt_pk_bf16_f32 v99, v102, v103\n\t"
  "buffer_store_dwordx4 v[96:99], v223, %8, s63 offen offset:0 sc1\n\t"
  "v_mfma_f32_16x16x32_bf16 v[124:127], a[92:95], v[188:191], v[124:127]\n\t"
  "v_cvt_pk_bf16_f32 v104, v104, v105\n\t"
  "v_cvt_pk_bf16_f32 v105, v106, v107\n\t"
  "v_cvt_pk_bf16_f32 v106, v108, v109\n\t"
  "v_cvt_pk_bf16_f32 v107, v110, v111\n\t"
  "buffer_store_dwordx4 v[104:107], v223, %8, s63 offen offset:1024 sc1\n\t"
  "v_cvt_pk_bf16_f32 v112, v112, v113\n\t"
  "v_cvt_pk_bf16_f32 v113, v114, v115\n\t"
  "v_cvt_pk_bf16_f32 v114, v116, v117\n\t"
  "v_cvt_pk_bf16_f32 v115, v118, v119\n\t"
  "buffer_store_dwordx4 v[112:115], v223, %8, s63 offen offset:2048 sc1\n\t"
  "v_cvt_pk_bf16_f32 v120, v120, v121\n\t"
  "v_cvt_pk_bf16_f32 v121, v122, v123\n\t"
  "v_cvt_pk_bf16_f32 v122, v124, v125\n\t"
  "v_cvt_pk_bf16_f32 v123, v126, v127\n\t"
  "buffer_store_dwordx4 v[120:123], v223, %8, s63 offen offset:3072 sc1\n\t"
  "v_lshrrev_b32_e32 v207, 4, v224\n\t"
  "v_and_b32_e32 v207, 48, v207\n\t"
  "v_lshl_add_u32 v212, v207, 6, v227\n\t"
  "v_add_u32_e32 v212, s40, v212\n\t"
  "s_mov_b64 exec, s[64:65]\n\t"
  "s_cbranch_execz LOS0%=\n\t"
  "ds_write_b128 v212, v[0:3] offset:0\n\t"
  "ds_write_b128 v212, v[8:11] offset:4096\n\t"
  "ds_write_b128 v212, v[16:19] offset:8192\n\t"
  "ds_write_b128 v212, v[24:27] offset:12288\n\t"
  "LOS0%=:\n\t"
  "s_mov_b64 exec, -1\n\t"
  "s_mov_b64 exec, s[66:67]\n\t"
  "s_cbranch_execz LOS1%=\n\t"
  "ds_write_b128 v212, v[32:35] offset:256\n\t"
  "ds_write_b128 v212, v[40:43] offset:4352\n\t"
  "ds_write_b128 v212, v[48:51] offset:8448\n\t"
  "ds_write_b128 v212, v[56:59] offset:12544\n\t"
  "LOS1%=:\n\t"
  "s_mov_b64 exec, -1\n\t"
  "s_mov_b64 exec, s[68:69]\n\t"
  "s_cbranch_execz LOS2%=\n\t"
  "ds_write_b128 v212, v[64:67] offset:512\n\t"
  "ds_write_b128 v212, v[72:75] offset:4608\n\t"
  "ds_write_b128 v212, v[80:83] offset:8704\n\t"
  "ds_write_b128 v212, v[88:91] offset:12800\n\t"
  "LOS2%=:\n\t"
  "s_mov_b64 exec, -1\n\t"
  "s_mov_b64 exec, s[70:71]\n\t"
  "s_cbranch_execz LOS3%=\n\t"
  "ds_write_b128 v212, v[96:99] offset:768\n\t"
  "ds_write_b128 v212, v[104:107] offset:4864\n\t"
  "ds_write_b128 v212, v[112:115] offset:8960\n\t"
  "ds_write_b128 v212, v[120:123] offset:13056\n\t"
  "LOS3%=:\n\t"
  "s_mov_b64 exec, -1\n\t"
  "s_waitcnt vmcnt(16)\n\t"
  "v_mfma_f32_16x16x32_bf16 v[248:251], v[236:239], a[160:163], v[248:251]\n\t"
  "v_mfma_f32_16x16x32_bf16 v[248:251], v[236:239], a[164:167], v[248:251]\n\t"
  "v_mfma_f32_16x16x32_bf16 v[248:251], v[236:239], a[168:171], v[248:251]\n\t"
  "v_mfma_f32_16x16x32_bf16 v[248:251], v[236:239], a[172:175], v[248:251]\n\t"
  "v_mfma_f32_16x16x32_bf16 v[248:251], v[236:239], a[176:179], v[248:251]\n\t"
  "v_mfma_f32_16x16x32_bf16 v[248:251], v[236:239], a[180:183], v[248:251]\n\t"
  "v_mfma_f32_16x16x32_bf16 v[248:251], v[236:239], a[184:187], v[248:251]\n\t"
  "v_mfma_f32_16x16x32_bf16 v[248:251], v[236:239], a[188:191], v[248:251]\n\t"
  "v_mfma_f32_16x16x32_bf16 v[252:255], v[232:235], a[192:195], 0\n\t"
  "v_mfma_f32_16x16x32_bf16 v[252:255], v[232:235], a[196:199], v[252:255]\n\t"
  "v_mfma_f32_16x16x32_bf16 v[252:255], v[232:235], a[200:203], v[252:255]\n\t"
  "v_mfma_f32_16x16x32_bf16 v[252:255], v[232:235], a[204:207], v[252:255]\n\t"
  "v_mfma_f32_16x16x32_bf16 v[252:255], v[232:235], a[208:211], v[252:255]\n\t"
  "v_mfma_f32_16x16x32_bf16 v[252:255], v[232:235], a[212:215], v[252:255]\n\t"
  "v_mfma_f32_16x16x32_bf16 v[252:255], v[232:235], a[216:219], v[252:255]\n\t"
  "v_mfma_f32_16x16x32_bf16 v[252:255], v[232:235], a[220:223], v[252:255]\n\t"
  "v_mfma_f32_16x16x32_bf16 v[252:255], v[236:239], a[224:227], v[252:255]\n\t"
  "v_mfma_f32_16x16x32_bf16 v[252:255], v[236:239], a[228:231], v[252:255]\n\t"
  "v_mfma_f32_16x16x32_bf16 v[252:255], v[236:239], a[232:235], v[252:255]\n\t"
  "v_mfma_f32_16x16x32_bf16 v[252:255], v[236:239], a[236:239], v[252:255]\n\t"
  "v_mfma_f32_16x16x32_bf16 v[252:255], v[236:239], a[240:243], v[252:255]\n\t"
  "v_mfma_f32_16x16x32_bf16 v[252:255], v[236:239], a[244:247], v[252:255]\n\t"
  "v_mfma_f32_16x16x32_bf16 v[252:255], v[236:239], a[248:251], v[252:255]\n\t"
  "v_mfma_f32_16x16x32_bf16 v[252:255], v[236:239], a[252:255], v[252:255]\n\t"
  "v_mfma_f32_16x16x32_bf16 v[192:195], v[232:235], a[128:131], 0\n\t"
  "v_mfma_f32_16x16x32_bf16 v[192:195], v[232:235], a[132:135], v[192:195]\n\t"
  "v_mfma_f32_16x16x32_bf16 v[192:195], v[232:235], a[136:139], v[192:195]\n\t"
  "v_mfma_f32_16x16x32_bf16 v[192:195], v[232:235], a[140:143], v[192:195]\n\t"
  "v_mfma_f32_16x16x32_bf16 v[192:195], v[232:235], a[144:147], v[192:195]\n\t"
  "v_mfma_f32_16x16x32_bf16 v[192:195], v[232:235], a[148:151], v[192:195]\n\t"
  "v_mfma_f32_16x16x32_bf16 v[192:195], v[232:235], a[152:155], v[192:195]\n\t"
  "v_mfma_f32_16x16x32_bf16 v[192:195], v[232:235], a[156:159], v[192:195]\n\t"
  "s_waitcnt lgkmcnt(0)\n\t"
  "s_nop 7\n\t"
  "s_branch LEND%=\n\t"
  "LBAIL%=:\n\t"
  "s_waitcnt vmcnt(0)\n\t"
  "s_barrier\n\t"
  "LEND%=:\n\t"
    : "=&s"(done), "=&{v[240:255]}"(ac16), "=&{v[192:195]}"(ac4), "=&{a[0:31]}"(xq), "+{v[216:231]}"(vin), "+{v[232:239]}"(vsel), "+{v[214:215]}"(cp)
    : "s"(srdH), "s"(rw), "s"(rx), "s"(ro), "{s[40:43]}"(si0), "{s[44:59]}"(sin)
    : "v0", "v1", "v2", "v3", "v4", "v5", "v6", "v7", "v8", "v9", "v10", "v11", "v12", "v13", "v14", "v15", "v16", "v17", "v18", "v19", "v20", "v21", "v22", "v23", "v24", "v25", "v26", "v27", "v28", "v29", "v30", "v31", "v32", "v33", "v34", "v35", "v36", "v37", "v38", "v39", "v40", "v41", "v42", "v43", "v44", "v45", "v46", "v47", "v48", "v49", "v50", "v51", "v52", "v53", "v54", "v55", "v56", "v57", "v58", "v59", "v60", "v61", "v62", "v63", "v64", "v65", "v66", "v67", "v68", "v69", "v70", "v71", "v72", "v73", "v74", "v75", "v76", "v77", "v78", "v79", "v80", "v81", "v82", "v83", "v84", "v85", "v86", "v87", "v88", "v89", "v90", "v91", "v92", "v93", "v94", "v95", "v96", "v97", "v98", "v99", "v100", "v101", "v102", "v103", "v104", "v105", "v106", "v107", "v108", "v109", "v110", "v111", "v112", "v113", "v114", "v115", "v116", "v117", "v118", "v119", "v120", "v121", "v122", "v123", "v124", "v125", "v126", "v127", "v128", "v129", "v130", "v131", "v132", "v133", "v134", "v135", "v136", "v137", "v138", "v139", "v140", "v141", "v142", "v143", "v144", "v145", "v146", "v147", "v148", "v149", "v150", "v151", "v152", "v153", "v154", "v155", "v156", "v157", "v158", "v159", "v160", "v161", "v162", "v163", "v164", "v165", "v166", "v167", "v168", "v169", "v170", "v171", "v172", "v173", "v174", "v175", "v176", "v177", "v178", "v179", "v180", "v181", "v182", "v183", "v184", "v185", "v186", "v187", "v188", "v189", "v190", "v191", "v196", "v197", "v198", "v199", "v200", "v201", "v202", "v203", "v204", "v205", "v206", "v207", "v208", "v209", "v210", "v211", "v212", "v213", "a32", "a33", "a34", "a35", "a36", "a37", "a38", "a39", "a40", "a41", "a42", "a43", "a44", "a45", "a46", "a47", "a48", "a49", "a50", "a51", "a52", "a53", "a54", "a55", "a56", "a57", "a58", "a59", "a60", "a61", "a62", "a63", "a64", "a65", "a66", "a67", "a68", "a69", "a70", "a71", "a72", "a73", "a74", "a75", "a76", "a77", "a78", "a79", "a80", "a81", "a82", "a83", "a84", "a85", "a86", "a87", "a88", "a89", "a90", "a91", "a92", "a93", "a94", "a95", "a96", "a97", "a98", "a99", "a100", "a101", "a102", "a103", "a104", "a105", "a106", "a107", "a108", "a109", "a110", "a111", "a112", "a113", "a114", "a115", "a116", "a117", "a118", "a119", "a120", "a121", "a122", "a123", "a124", "a125", "a126", "a127", "a128", "a129", "a130", "a131", "a132", "a133", "a134", "a135", "a136", "a137", "a138", "a139", "a140", "a141", "a142", "a143", "a144", "a145", "a146", "a147", "a148", "a149", "a150", "a151", "a152", "a153", "a154", "a155", "a156", "a157", "a158", "a159", "a160", "a161", "a162", "a163", "a164", "a165", "a166", "a167", "a168", "a169", "a170", "a171", "a172", "a173", "a174", "a175", "a176", "a177", "a178", "a179", "a180", "a181", "a182", "a183", "a184", "a185", "a186", "a187", "a188", "a189", "a190", "a191", "a192", "a193", "a194", "a195", "a196", "a197", "a198", "a199", "a200", "a201", "a202", "a203", "a204", "a205", "a206", "a207", "a208", "a209", "a210", "a211", "a212", "a213", "a214", "a215", "a216", "a217", "a218", "a219", "a220", "a221", "a222", "a223", "a224", "a225", "a226", "a227", "a228", "a229", "a230", "a231", "a232", "a233", "a234", "a235", "a236", "a237", "a238", "a239", "a240", "a241", "a242", "a243", "a244", "a245", "a246", "a247", "a248", "a249", "a250", "a251", "a252", "a253", "a254", "a255", "s60", "s61", "s62", "s63", "s64", "s65", "s66", "s67", "s68", "s69", "s70", "s71", "s72", "s73", "s74", "s75", "s76", "s77", "s78", "s79", "s80", "s81", "s82", "s83", "s84", "s85", "s86", "s87", "s88", "s89", "s90", "s91", "vcc", "scc", "memory");
  }
  asm volatile("" : "+s"(bid), "+s"(w));
  GEOMETRY
  const srd_t rh = mksrd(ws + WS_H, N * 256);
  char* tile = lds + 131072 + w * 8192;
  const unsigned vKb0 = vin[0], vKb1 = vin[1], vVb0 = vin[2], vVb1 = vin[3], vL16 = vin[8], vCn = vin[10], rowoff = vin[14], pofs = vin[15];
  const unsigned lane = vL16 >> 4, m = lane & 15, g = lane >> 4, lflag = (vin[9] - WS_SYN) >> 7, pflag = (cp[1] - WS_SYN) >> 7;
  const u32x4 sel[2] = {u32x4{vsel[0], vsel[1], vsel[2], vsel[3]}, u32x4{vsel[4], vsel[5], vsel[6], vsel[7]}};
  unsigned cwl = cp[0];
  u32x4 xv[4]; f32x4 acc[(NG + 1) / 2], ol[8], o[8];
#pragma unroll
  for (int c = 0; c < 4; ++c) xv[c] = u32x4{xq[4 * c], xq[4 * c + 1], xq[4 * c + 2], xq[4 * c + 3]};
#pragma unroll
  for (int p = 0; p < (NG + 1) / 2; ++p) acc[p] = p < 4 ? f32x4{ac16[4 * p], ac16[4 * p + 1], ac16[4 * p + 2], ac16[4 * p + 3]} : ac4;
#pragma unroll
  for (int db = 0; db < 8; ++db) ol[db] = f32x4{0.f, 0.f, 0.f, 0.f};
  if (done) {
    f32x4 sc[2];
#pragma unroll
    for (int t = 0; t < 2; ++t) { sc[t] = f32x4{0.f, 0.f, 0.f, 0.f};
#pragma unroll
      for (int ks = 0; ks < 4; ++ks) sc[t] = MFMA16(*LDSP(const u32x4, (t ? vKb1 : vKb0) + 0xc000u + 512u * ks), (u32x4{xq[16 + 4 * ks], xq[17 + 4 * ks], xq[18 + 4 * ks], xq[19 + 4 * ks]}), sc[t]);
#pragma unroll
      for (int i = 0; i < 4; ++i) sc[t][i] = __builtin_amdgcn_exp2f(sc[t][i]); }
    const u32x4 pl = pack8(sc[0], sc[1]);
#pragma unroll
    for (int db = 0; db < 8; ++db) ol[db] = MFMA16(tr_frag(vVb0 + 0xc000u, vVb1 + 0xc000u, db, 0), pl, ol[db]);
  } else {
    attn_general(ws, ws, lds, qblk, split, lane, w);
#pragma unroll
    for (unsigned i = 0; i < 16; ++i) dma16(rw, WS_W + (w * 16 + i) * 1024u + vL16, ldsb + 65536u + (w * 16 + i) * 1024u);
#pragma unroll
    for (int c = 0; c < 4; ++c) xv[c] = bld4(rx, rowoff + 64 * c);
  }
  asm volatile("s_waitcnt vmcnt(0) lgkmcnt(0)\n\ts_barrier" ::: "memory");
  if (w == 0 && lane == 0) set_flag(ws, qblk * NSPLIT + split);
  if (done != 1) {
    wait_flags(ws, lflag, 0u);
    *(u32x4*)(tile + 4096 + vL16) = ld4_dev(rw, vL16, lofs); *(u32x4*)(tile + 5120 + vL16) = ld4_dev(rw, vL16 + 1024, lofs);
    cwl = __builtin_amdgcn_raw_buffer_load_b32(rw, vCn, 0, 16);
    WAVE_SYNC();
  }
#pragma unroll
  for (int p = 0; p < (NG + 1) / 2; ++p) {
    if (done != 1) acc[p] = f32x4{0.f, 0.f, 0.f, 0.f};
#pragma unroll 1
    for (int j = 2 * p; j < 2 * p + 2 && PA + j < (int)nr; ++j) {
      const Idx ix = ld_idx(tile + 4096, PA + j, 2 * g); const u32x4 sl = j & 1 ? sel[1] : sel[0]; u32x4 t[GB];
      if (done != 1) {
#pragma unroll
        for (int k = 0; k < GL; ++k) t[k] = gload(rh, ix, k, m * 16);
#pragma unroll
        for (int k = 0; k < GL; ++k) acc[p] = MFMA16(sl, t[k], acc[p]); }
#pragma unroll
      for (int k = GL; k < GB; ++k) t[k] = gload(rh, ix, k, m * 16);
#pragma unroll
      for (int k = GL; k < GB; ++k) acc[p] = MFMA16(sl, t[k], acc[p]);
      row_rest(rh, ws, sl, ix, r0 + PA + j, __builtin_amdgcn_readlane(cwl, PA + j) & 127u, m * 16, g, acc[p]); }
    const float dv = bfhi(g < 2 ? __builtin_amdgcn_readlane(cwl, PA + 2 * p) : __builtin_amdgcn_readlane(cwl, PA + 2 * p + 1));
    *(u32x2*)(tile + (PA + 2 * p + (g >> 1)) * 272 + m * 16 + (g & 1) * 8) = u32x2{cvtpk(acc[p][0] * dv, acc[p][1] * dv), cvtpk(acc[p][2] * dv, acc[p][3] * dv)}; }
  wait_flags(ws, pflag, 0u);
  *(u32x4*)(tile + g * 272 + m * 16) = ld4_dev(rw, vL16, WS_AGG + r0 * 256u);
  WAVE_SYNC();
  const char* wl = lds + 65536 + vL16;
#pragma unroll
  for (int c = 0; c < 8; ++c) o[c] = f32x4{bflo(xv[c >> 1][2 * (c & 1)]), bfhi(xv[c >> 1][2 * (c & 1)]), bflo(xv[c >> 1][2 * (c & 1) + 1]), bfhi(xv[c >> 1][2 * (c & 1) + 1])};
#pragma unroll
  for (int j = 0; j < 4; ++j)
#pragma unroll
    for (int c = 0; c < 8; ++c) o[c] = MFMA16(*(const u32x4*)(wl + 32768 + (c * 4 + j) * 1024), *(const u32x4*)(tile + m * 272 + (4 * j + g) * 16), o[c]);
  float wt[NSPLIT] = {1.f, 1.f, 1.f, 1.f, 1.f}, il = 1.f;
  const unsigned cu = g == m >> 2 ? 0x3f80u << (16 * (m & 1)) : 0u, c0 = m & 2 ? 0u : cu, c1 = m & 2 ? cu : 0u;
  if (!done) {
    float mm = -INFINITY;
#pragma unroll
    for (int i = 0; i < NSPLIT; ++i) { wt[i] = __uint_as_float(__builtin_amdgcn_raw_buffer_load_b32(rw, WS_ML + (pso[i] * 16 + r0 + min(m, nr - 1)) * 4u, 0, 16)); mm = fmaxf(mm, wt[i]); }
#pragma unroll
    for (int i = 0; i < NSPLIT; ++i) wt[i] = exp2f(wt[i] - mm);
  }
#pragma unroll
  for (int j = 3; j >= 0; --j) {
#pragma unroll
    for (int k = 0; k < NSPLIT; ++k) { const int i = (k + NSPLIT - 1) % NSPLIT;
      const u32x4 po = k == 0 && done ? *(const u32x4*)(lds + 0x2000 + j * 4096 + g * 1024 + (GR * w + m) * 16) : ld4_dev(rw, pofs + j * 1024, WS_PO - WS_QF + pso[i] * 4096u);
      if (done) { ol[2 * j] = MFMA16((u32x4{c0, c1, 0u, 0u}), po, ol[2 * j]); ol[2 * j + 1] = MFMA16((u32x4{0u, 0u, c0, c1}), po, ol[2 * j + 1]); }
      else { ol[2 * j] += f32x4{bflo(po[0]), bfhi(po[0]), bflo(po[1]), bfhi(po[1])} * wt[i]; ol[2 * j + 1] += f32x4{bflo(po[2]), bfhi(po[2]), bflo(po[3]), bfhi(po[3])} * wt[i]; } }
    if (j == 3) il = __builtin_amdgcn_rcpf(gsum(g == 3 ? ol[7][3] : 0.f));
    const u32x4 z = pack8(ol[2 * j] * il, ol[2 * j + 1] * il);
#pragma unroll
    for (int c = 0; c < 8; ++c) o[c] = MFMA16(*(const u32x4*)(wl + (c * 4 + j) * 1024), z, o[c]);
  }
#pragma unroll
  for (int c = 0; c < 4; ++c) __builtin_amdgcn_raw_buffer_store_b128(pack8(o[2 * c], o[2 * c + 1]), ro, rowoff + 64 * c, 0, 0);
}

extern "C" void kernel_launch(void* const* d_in, const int* in_sizes, int n_in, void* d_out, int out_size, void* d_ws, size_t ws_size, hipStream_t stream) {
  const uint16_t* x = (const uint16_t*)d_in[0];
  const int* src = (const int*)d_in[1];
  const int* dst = (const int*)d_in[2];
  const uint16_t* deg_inv = (const uint16_t*)d_in[3];
  const uint16_t* w_msg = (const uint16_t*)d_in[4];
  const uint16_t* wq = (const uint16_t*)d_in[5];
  const uint16_t* wk = (const uint16_t*)d_in[6];
  const uint16_t* wv = (const uint16_t*)d_in[7];
  const uint16_t* w_out = (const uint16_t*)d_in[8];
  char* ws = (char*)d_ws;
  k_pre<<<PRE_ROWWG + PRE_WWG + NCH / 2, 256, 0, stream>>>(x, src, dst, ws, wq, wk, wv, w_out, w_msg);
  k_attn<<<GRID_ATTN, 256, 0, stream>>>(x, deg_inv, ws, (uint16_t*)d_out);
}
```
